```python
import torch

def _softmax(x, axis=-1):
    return torch.softmax(x, dim=axis)
def _gelu(x):
    return torch.nn.functional.gelu(x, approximate="tanh")
def _silu(x):
    return torch.nn.functional.silu(x)
def _sigmoid(x):
    return torch.sigmoid(x)
def _layernorm(x, axis=-1, eps=1e-5):
    m = x.mean(dim=axis, keepdim=True)
    v = x.var(dim=axis, keepdim=True, unbiased=False)
    return (x - m) / torch.sqrt(v + eps)
def _scatter_add(tgt, idx, src, axis=0):
    return tgt.clone().index_add_(axis, idx, src)
def _segment_sum(src, seg, n):
    out = torch.zeros((n,) + src.shape[1:], dtype=src.dtype)
    return out.index_add_(0, seg, src)
def _pad(x, pw):
    flat = tuple(v for pair in reversed(pw) for v in pair)
    return torch.nn.functional.pad(x, flat)

kernel_name = 'sequential_preLN__sliding_block__mixer_token_mlp__prompt'

def setup_inputs():

    _w = (lambda a: (lambda t: t.to(torch.bfloat16) if t.is_floating_point() else t)(torch.as_tensor(a)))
    def _A_setup():
        B, S, D = 1, 2048, 576
        return {
            "x": _w((lambda *s: torch.randn(*s, dtype=torch.bfloat16) * 0.05)(B, S, D)),
            "wqkv": _w((lambda *s: torch.randn(*s, dtype=torch.bfloat16) * 0.05)(D, 3 * D)),
            "wo": _w((lambda *s: torch.randn(*s, dtype=torch.bfloat16) * 0.05)(D, D)),
        }
    _A_in = _A_setup()

    def _B_setup():
        B, S, D, F = 1, 2048, 576, 4608
        return {
            "x": _w((lambda *s: torch.randn(*s, dtype=torch.bfloat16) * 0.05)(B, S, D)),
            "w1": _w((lambda *s: torch.randn(*s, dtype=torch.bfloat16) * 0.8)(S, F) * (S ** -0.5)),
            "b1": _w((lambda *s: torch.randn(*s, dtype=torch.bfloat16) * 0.8)(F) * 0.02),
            "w2": _w((lambda *s: torch.randn(*s, dtype=torch.bfloat16) * 0.8)(F, S) * (F ** -0.5)),
            "b2": _w((lambda *s: torch.randn(*s, dtype=torch.bfloat16) * 0.8)(S) * 0.02),
        }
    _B_in = _B_setup()

    return {
        "A_x": _A_in["x"],
        "A_wqkv": _A_in["wqkv"],
        "A_wo": _A_in["wo"],
        "B_x": _B_in["x"],
        "B_w1": _B_in["w1"],
        "B_b1": _B_in["b1"],
        "B_w2": _B_in["w2"],
        "B_b2": _B_in["b2"],
    }

def reference(A_x, A_wqkv, A_wo, B_x, B_w1, B_b1, B_w2, B_b2):
    def _A_ref(x, wqkv, wo):
        B, S, D = x.shape
        H = 32; Dh = D // H; W = 256
        qkv = (lambda x, *d: x.permute(*d))((x @ wqkv).reshape(B, S, 3, H, Dh), 2, 0, 3, 1, 4)
        q, k, v = qkv[0], qkv[1], qkv[2]
        scores = torch.einsum("bhqd,bhkd->bhqk", q, k) * (Dh ** -0.5)
        idx = torch.arange(S)
        mask = (idx[:, None] - idx[None, :] >= 0) & (idx[:, None] - idx[None, :] < W)
        scores = torch.where(mask[None, None], scores, -1e9)
        p = _softmax(scores, axis=-1)
        o = torch.einsum("bhqk,bhkd->bhqd", p, v)
        return (lambda x, *d: x.permute(*d))(o, 0, 2, 1, 3).reshape(B, S, D) @ wo

    def _B_ref(x, w1, b1, w2, b2):
        B, S, D = x.shape
        h = _layernorm(x, axis=-1)
        u = torch.einsum("bsd,sf->bdf", h, w1) + b1
        u = _gelu(u)
        y = torch.einsum("bdf,fs->bds", u, w2) + b2
        return (lambda x, *d: x.permute(*d))(y, 0, 2, 1)

    x = A_x
    h = x + _A_ref(_layernorm(x, axis=-1), A_wqkv, A_wo)
    h2 = h + _B_ref(_layernorm(h, axis=-1), B_w1, B_b1, B_w2, B_b2)
    return h2
```

```cpp
#include <hip/hip_runtime.h>
#include <hip/hip_bf16.h>
#include <stdint.h>
typedef unsigned short u16;
constexpr int S=2048, D=576, F=4608, H=32, DH=18, W=256;
__device__ __forceinline__ float bf2f(u16 v){ return __uint_as_float(((unsigned)v)<<16); }
__device__ __forceinline__ u16 f2bf(float f){ unsigned u=__float_as_uint(f); return (u16)((u + 0x7FFF + ((u>>16)&1))>>16); }

__global__ void ln_k(const u16* x, u16* y, int twice){
  int row=blockIdx.x; int t=threadIdx.x;
  float v[9]; float s=0;
  for(int i=0;i<9;i++){ v[i]=bf2f(x[row*D+t+64*i]); s+=v[i]; }
  for(int rep=0; rep<=twice; rep++){
    s=0; for(int i=0;i<9;i++) s+=v[i];
    for(int o=32;o>0;o>>=1) s+=__shfl_xor(s,o);
    float m=s/D; float q=0;
    for(int i=0;i<9;i++){ float d=v[i]-m; q+=d*d; }
    for(int o=32;o>0;o>>=1) q+=__shfl_xor(q,o);
    float r=rsqrtf(q/D+1e-5f);
    for(int i=0;i<9;i++) v[i]=(v[i]-m)*r;
  }
  for(int i=0;i<9;i++) y[row*D+t+64*i]=f2bf(v[i]);
}
template<int MODE>
__global__ void gemm_k(const u16* A, long sam, long sak, const u16* B, int M,int N,int K, u16* C, const u16* bias, const u16* res){
  __shared__ float As[16][17], Bs[16][17];
  int tx=threadIdx.x&15, ty=threadIdx.x>>4;
  int m=blockIdx.y*16+ty, n=blockIdx.x*16+tx;
  float acc=0;
  for(int k0=0;k0<K;k0+=16){
    As[ty][tx]=bf2f(A[(long)(blockIdx.y*16+ty)*sam+(long)(k0+tx)*sak]);
    Bs[ty][tx]=bf2f(B[(long)(k0+ty)*N+n]);
    __syncthreads();
    for(int k=0;k<16;k++) acc+=As[ty][k]*Bs[k][tx];
    __syncthreads();
  }
  if(MODE==0) C[(long)m*N+n]=f2bf(acc);
  if(MODE==1) C[(long)m*N+n]=f2bf(acc+bf2f(res[(long)m*N+n]));
  if(MODE==2){ float u=acc+bf2f(bias[n]); float g=0.5f*u*(1.f+tanhf(0.7978845608f*(u+0.044715f*u*u*u))); C[(long)m*N+n]=f2bf(g);}
  if(MODE==3){ float y=acc+bf2f(bias[n]); C[(long)n*M+m]=f2bf(y+bf2f(res[(long)n*M+m])); }
}
__global__ void attn_k(const u16* qkv, u16* o){
  int h=blockIdx.y; int q=blockIdx.x*64+threadIdx.x;
  float qv[DH]; for(int d=0;d<DH;d++) qv[d]=bf2f(qkv[(long)q*3*D+h*DH+d]);
  float m=-1e30f,l=0,acc[DH]; for(int d=0;d<DH;d++) acc[d]=0;
  int k0=q-W+1; if(k0<0)k0=0;
  for(int k=k0;k<=q;k++){
    float s=0; for(int d=0;d<DH;d++) s+=qv[d]*bf2f(qkv[(long)k*3*D+D+h*DH+d]);
    s*=0.23570226f;
    float mn=fmaxf(m,s); float a=__expf(m-mn), p=__expf(s-mn);
    l=l*a+p; for(int d=0;d<DH;d++) acc[d]=acc[d]*a+p*bf2f(qkv[(long)k*3*D+2*D+h*DH+d]);
    m=mn;
  }
  for(int d=0;d<DH;d++) o[(long)q*D+h*DH+d]=f2bf(acc[d]/l);
}
extern "C" void kernel_launch(void* const* d_in, const int* in_sizes, int n_in, void* d_out, int out_size, void* d_ws, size_t ws_size, hipStream_t stream){
  const u16* x=(const u16*)d_in[0]; const u16* wqkv=(const u16*)d_in[1]; const u16* wo=(const u16*)d_in[2];
  const u16* w1=(const u16*)d_in[4]; const u16* b1=(const u16*)d_in[5]; const u16* w2=(const u16*)d_in[6]; const u16* b2=(const u16*)d_in[7];
  u16* ws=(u16*)d_ws; u16* xn=ws; u16* qkv=xn+S*D; u16* o=qkv+S*3*D; u16* h=o+S*D; u16* hn=h+S*D; u16* u=hn+S*D;
  ln_k<<<S,64,0,stream>>>(x,xn,0);
  gemm_k<0><<<dim3(3*D/16,S/16),256,0,stream>>>(xn,D,1,wqkv,S,3*D,D,qkv,nullptr,nullptr);
  attn_k<<<dim3(S/64,H),64,0,stream>>>(qkv,o);
  gemm_k<1><<<dim3(D/16,S/16),256,0,stream>>>(o,D,1,wo,S,D,D,h,nullptr,x);
  ln_k<<<S,64,0,stream>>>(h,hn,1);
  gemm_k<2><<<dim3(F/16,D/16),256,0,stream>>>(hn,1,D,w1,D,F,S,u,b1,nullptr);
  gemm_k<3><<<dim3(S/16,D/16),256,0,stream>>>(u,F,1,w2,D,S,F,(u16*)d_out,b2,h);
}
```

```cpp
#include <hip/hip_runtime.h>
#include <hip/hip_bf16.h>
#include <stdint.h>

typedef unsigned short u16;
typedef unsigned long long u64;
typedef __attribute__((ext_vector_type(8))) __bf16 bf16x8;
typedef __attribute__((ext_vector_type(4))) __bf16 bf16x4;
typedef __attribute__((ext_vector_type(2))) __bf16 bf16x2;
typedef __attribute__((ext_vector_type(4))) short s16x4;
typedef __attribute__((ext_vector_type(4))) float f32x4;
typedef __attribute__((ext_vector_type(2))) float f32x2;
typedef __attribute__((ext_vector_type(16))) float f32x16;
typedef __attribute__((ext_vector_type(4))) unsigned u32x4;
typedef __attribute__((ext_vector_type(2))) unsigned u32x2;
#define LDS3 __attribute__((address_space(3)))
#define DEV __device__ __forceinline__

constexpr int S_ = 2048, D_ = 576, F_ = 4608;
constexpr size_t WS_FLAGS = 0;
constexpr size_t WS_STATS = 65536;
constexpr size_t WS_ZERO_BYTES = 65536 + 196608;
constexpr size_t WS_QKVT = 262144;
constexpr size_t WS_OMAIN = WS_QKVT + (size_t)1728 * 2048 * 2;
constexpr size_t WS_OTAIL = WS_OMAIN + (size_t)128 * 16 * 1024;
constexpr size_t WS_HT = WS_OTAIL + (size_t)32 * 2048 * 4;
constexpr size_t WS_HNT = WS_HT + (size_t)64 * 36 * 1024;
constexpr size_t WS_UT = WS_HNT + (size_t)64 * 36 * 1024;
constexpr size_t WS_PART = WS_UT + (size_t)144 * 36 * 1024;
constexpr size_t WS_END = WS_PART + (size_t)64 * 16 * 9 * 1024;

#define STAMP(ph, i)
#define PROF_ARG
#define PROF_PASS

template <int OFF>
DEV void glds(const void* sbase, unsigned voff, unsigned lds) {
  asm volatile("s_mov_b32 m0, %2\n\ts_nop 0\n\tglobal_load_lds_dwordx4 %0, %1 offset:%3" ::"v"(voff), "s"(sbase), "s"(lds), "i"(OFF) : "memory");
}
template <int N> DEV void wait_vm() { asm volatile("s_waitcnt vmcnt(%0)" ::"i"(N) : "memory"); }
DEV void wait_lgkm0() { asm volatile("s_waitcnt lgkmcnt(0)" ::: "memory"); }
DEV void bar() { asm volatile("" ::: "memory"); __builtin_amdgcn_s_barrier(); asm volatile("" ::: "memory"); }
DEV bf16x8 lds_b128(unsigned a) { return *(const LDS3 bf16x8*)a; }
DEV bf16x4 lds_tr(unsigned a) { return __builtin_amdgcn_ds_read_tr16_b64_v4bf16((LDS3 bf16x4*)a); }
DEV bf16x8 cat(bf16x4 a, bf16x4 b) { return __builtin_shufflevector(a, b, 0, 1, 2, 3, 4, 5, 6, 7); }
DEV f32x4 mfma16(bf16x8 a, bf16x8 b, f32x4 c) { return __builtin_amdgcn_mfma_f32_16x16x32_bf16(a, b, c, 0, 0, 0); }
DEV unsigned pk2(float a, float b) { bf16x2 p = {(__bf16)a, (__bf16)b}; return __builtin_bit_cast(unsigned, p); }
DEV float bflo(unsigned u) { return __uint_as_float(u << 16); }
DEV float bfhi(unsigned u) { return __uint_as_float(u & 0xffff0000u); }
DEV void st16_wt(void* p, u32x4 v) { asm volatile("global_store_dwordx4 %0, %1, off sc1\n\ts_nop 1" ::"v"(p), "v"(v) : "memory"); }
DEV void st8_wt(void* p, u32x2 v) { asm volatile("global_store_dwordx2 %0, %1, off sc1\n\ts_nop 1" ::"v"(p), "v"(v) : "memory"); }
DEV void st4_wt(void* p, unsigned v) { asm volatile("global_store_dword %0, %1, off sc1\n\ts_nop 1" ::"v"(p), "v"(v) : "memory"); }
template <int CTRL> DEV float dpp_add(float v) {
  return v + __builtin_bit_cast(float, __builtin_amdgcn_update_dpp(0, __builtin_bit_cast(int, v), CTRL, 0xf, 0xf, true));
}
DEV float row16_sum(float v) { v = dpp_add<0x128>(v); v = dpp_add<0x124>(v); v = dpp_add<0x122>(v); v = dpp_add<0x121>(v); return v; }
DEV float dot2(unsigned a, unsigned b, float c) {
  return __builtin_amdgcn_fdot2_f32_bf16(__builtin_bit_cast(bf16x2, a), __builtin_bit_cast(bf16x2, b), c, false);
}

constexpr int K1_NS = 4, K1_STAGE = 32768, K1_NST = 9;
constexpr int K1_AUX = K1_NS * K1_STAGE;
constexpr int K1_LDS = K1_AUX + 1024 + 16 * 512;
__global__ __launch_bounds__(512, 2) void k_qkv(const u16* __restrict__ x, const u16* __restrict__ wqkv, char* __restrict__ ws PROF_ARG) {
  extern __shared__ __attribute__((aligned(16))) char smem[];
  STAMP(0, 0);
  const int wg = blockIdx.x, tid = threadIdx.x, l = tid & 63;
  const int wid = __builtin_amdgcn_readfirstlane(tid >> 6);
  if (wg >= 224) {
    u32x4 z = {0, 0, 0, 0};
    for (unsigned i = (wg - 224) * 512 + tid; i < WS_ZERO_BYTES / 16; i += 32 * 512) ((u32x4*)ws)[i] = z;
    return;
  }
  const int xcd = wg & 7, idx = wg >> 3;
  const int tb = (xcd & 3) * 4 + (idx & 3), ct = (xcd >> 2) * 7 + (idx >> 2);
  const unsigned sb = (unsigned)(size_t)smem;
  if (wid >= 4) {
    const int lw = wid - 4;
    const unsigned vx = (8 * lw + (l >> 3)) * 1152 + (((l & 7) ^ (((l >> 4) & 1) | (lw << 1))) * 16);
    const int fw = (l >> 4) | (((lw >> 1) & 1) << 2);
    int cw = (l & 15) ^ (fw << 1);
    const int cw_real = cw;
    if (ct == 13) cw &= 7;
    const unsigned vw = (4 * lw + (l >> 4)) * 3456 + cw * 16;
    const char* bx = (const char*)x + (size_t)tb * 128 * 1152;
    const char* bw = (const char*)wqkv + ct * 256;
    auto issue = [&](int s) {
      const unsigned slot = sb + (s & (K1_NS - 1)) * K1_STAGE + lw * 1024;
      const char* px = bx + s * 128;
      const char* pw = bw + (size_t)s * 64 * 3456;
#pragma unroll
      for (int i = 0; i < 4; ++i) glds<0>(px + i * 32 * 1152, vx, slot + i * 4096);
#pragma unroll
      for (int i = 0; i < 4; ++i) glds<0>(pw + i * 16 * 3456, vw, slot + 16384 + i * 4096);
    };
    issue(0); issue(1); issue(2);
    float sx[4] = {0, 0, 0, 0}, sq[4] = {0, 0, 0, 0}, cs[8] = {0, 0, 0, 0, 0, 0, 0, 0};
    unsigned klo = 0x00003f80u, khi = 0x3f800000u;
    asm volatile("" : "+s"(klo), "+s"(khi));
    wait_vm<16>();
    bar();
    for (int s = 0; s < K1_NST; ++s) {
      if (s + 3 < K1_NST) issue(s + 3);
      const unsigned slot = sb + (s & (K1_NS - 1)) * K1_STAGE + lw * 1024 + l * 16;
#pragma unroll
      for (int i = 0; i < 4; ++i) {
        u32x4 v = *(const LDS3 u32x4*)(slot + i * 4096);
        u32x4 w = *(const LDS3 u32x4*)(slot + 16384 + i * 4096);
#pragma unroll
        for (int e = 0; e < 4; ++e) {
          sx[i] = dot2(v[e], 0x3f803f80u, sx[i]);
          sq[i] = dot2(v[e], v[e], sq[i]);
          cs[2 * e] = dot2(w[e], klo, cs[2 * e]);
          cs[2 * e + 1] = dot2(w[e], khi, cs[2 * e + 1]);
        }
      }
      if (s == K1_NST - 1) {
#pragma unroll
        for (int i = 0; i < 4; ++i) {
          float a = sx[i], b = sq[i];
          a = dpp_add<0xb1>(a); a = dpp_add<0x4e>(a); a = dpp_add<0x141>(a);
          b = dpp_add<0xb1>(b); b = dpp_add<0x4e>(b); b = dpp_add<0x141>(b);
          float mu = a * (1.f / 576.f);
          float var = fmaxf(b * (1.f / 576.f) - mu * mu, 0.f);
          float r = __builtin_amdgcn_rsqf(var + 1e-5f);
          if ((l & 7) == 0) *(LDS3 f32x2*)(sb + K1_AUX + (32 * i + 8 * lw + (l >> 3)) * 8) = (f32x2){mu, r};
        }
        const unsigned ca = sb + K1_AUX + 1024 + (lw * 4 + (l >> 4)) * 512 + cw_real * 32;
        *(LDS3 f32x4*)(ca) = (f32x4){cs[0], cs[1], cs[2], cs[3]};
        *(LDS3 f32x4*)(ca + 16) = (f32x4){cs[4], cs[5], cs[6], cs[7]};
      }
      if (s + 3 < K1_NST) wait_vm<16>(); else if (s + 2 < K1_NST) wait_vm<8>(); else wait_vm<0>();
      wait_lgkm0();
      bar();
    }
    return;
  }
  const int mh = wid >> 1, nh = wid & 1, i16 = l & 15, g = l >> 4, q = (l & 15) >> 2, p = l & 3;
  unsigned ax[2];
#pragma unroll
  for (int ks = 0; ks < 2; ++ks) ax[ks] = (64 * mh + 8 * (i16 >> 2) + (i16 & 3)) * 128 + (((4 * ks + g) ^ (i16 >> 1)) * 16);
  unsigned aw[4];
  const int f = q | ((g & 1) << 2);
#pragma unroll
  for (int c = 0; c < 4; ++c) aw[c] = 16384 + (8 * g + q) * 256 + (((4 * nh + c) ^ f) * 32) + 8 * p;
  f32x4 acc[4][4];
#pragma unroll
  for (int m = 0; m < 4; ++m)
#pragma unroll
    for (int c = 0; c < 4; ++c) acc[m][c] = (f32x4){0, 0, 0, 0};
  bf16x8 xa[2][4], wb[2][4];
  auto rd = [&](int ks, unsigned slot) {
#pragma unroll
    for (int m = 0; m < 4; ++m) xa[ks][m] = lds_b128(slot + ax[ks] + (m >> 1) * 4096 + (m & 1) * 512);
#pragma unroll
    for (int c = 0; c < 4; ++c) wb[ks][c] = cat(lds_tr(slot + aw[c] + ks * 8192), lds_tr(slot + aw[c] + ks * 8192 + 1024));
  };
  auto mm = [&](int ks) {
#pragma unroll
    for (int m = 0; m < 4; ++m)
#pragma unroll
      for (int c = 0; c < 4; ++c) acc[m][c] = mfma16(xa[ks][m], wb[ks][c], acc[m][c]);
  };
  bar();
  STAMP(0, 1);
  rd(0, sb); rd(1, sb); mm(0);
  wait_lgkm0(); bar();
  for (int s = 1; s < K1_NST; ++s) {
    const unsigned slot = sb + (s & (K1_NS - 1)) * K1_STAGE;
    rd(0, slot); mm(1);
    __builtin_amdgcn_sched_barrier(0);
    rd(1, slot); mm(0);
    wait_lgkm0(); bar();
  }
  mm(1);
  STAMP(0, 2);
  if (ct == 13 && nh == 1) return;
  float csum[4];
#pragma unroll
  for (int c = 0; c < 4; ++c) {
    float t = 0;
#pragma unroll
    for (int k = 0; k < 4; ++k) t += *(const LDS3 float*)(sb + K1_AUX + 1024 + (4 * g + k) * 512 + (64 * nh + 16 * c + i16) * 4);
    t += __shfl_xor(t, 16);
    t += __shfl_xor(t, 32);
    csum[c] = t;
  }
  char* qkvT = ws + WS_QKVT;
#pragma unroll
  for (int a = 0; a < 2; ++a) {
    f32x4 st[4];
#pragma unroll
    for (int k = 0; k < 4; ++k) st[k] = *(const LDS3 f32x4*)(sb + K1_AUX + (64 * mh + 32 * a + 8 * g + 2 * k) * 8);
#pragma unroll
    for (int c = 0; c < 4; ++c) {
      const int n = 128 * ct + 64 * nh + 16 * c + i16;
      const float sc = n < 576 ? 0.23570226039551584f * 1.4426950408889634f : 1.f;
      float v[8];
#pragma unroll
      for (int e = 0; e < 8; ++e) {
        float mu = st[e >> 1][(e & 1) * 2], r = st[e >> 1][(e & 1) * 2 + 1];
        v[e] = (acc[2 * a + (e >> 2)][c][e & 3] - mu * csum[c]) * (r * sc);
      }
      u32x4 o = {pk2(v[0], v[1]), pk2(v[2], v[3]), pk2(v[4], v[5]), pk2(v[6], v[7])};
      st16_wt(qkvT + (size_t)n * 4096 + (128 * tb + 64 * mh + 32 * a + 8 * g) * 2, o);
    }
  }
  STAMP(0, 3);
}

constexpr int A_KS = 1088, A_VS = 1040, A_QS = 576;
constexpr int A_K = 0, A_V = 20 * A_KS, A_Q = A_V + 19 * A_VS, A_LDS = A_Q + 18 * A_QS;
__global__ __launch_bounds__(512, 2) void k_attn(char* __restrict__ ws PROF_ARG) {
  extern __shared__ __attribute__((aligned(16))) char smem[];
  STAMP(1, 0);
  const int wg = blockIdx.x, tid = threadIdx.x, l = tid & 63;
  const int wid = __builtin_amdgcn_readfirstlane(tid >> 6);
  const int head = (wg & 7) * 4 + ((wg >> 3) & 3), tb = wg >> 5;
  const unsigned sb = (unsigned)(size_t)smem;
  const char* qkvT = ws + WS_QKVT;
  {
    int key0 = 256 * tb - 256;
    unsigned vk = l * 16;
    if (tb == 0) { key0 = 0; vk = (l & 31) * 16; }
    const char* bk = qkvT + (size_t)(576 + 18 * head) * 4096 + key0 * 2;
    const char* bv = bk + (size_t)576 * 4096;
    const char* bq = qkvT + (size_t)(18 * head) * 4096 + tb * 512;
    for (int r = wid; r < 18; r += 8) {
      glds<0>(bk + r * 4096, vk, sb + A_K + r * A_KS);
      if (l < 32) glds<0>(bq + r * 4096, l * 16, sb + A_Q + r * A_QS);
    }
    for (int r = 7 - wid; r < 18; r += 8) glds<0>(bv + r * 4096, vk, sb + A_V + r * A_VS);
    if (tid < 256) {
      *(LDS3 unsigned*)(sb + A_K + 18 * A_KS + tid * 4) = (tb == 0 && tid < 128) ? 0xf149f149u : 0u;
      *(LDS3 unsigned*)(sb + A_K + 19 * A_KS + tid * 4) = 0u;
    } else {
      *(LDS3 unsigned*)(sb + A_V + 18 * A_VS + (tid - 256) * 4) = 0x3f803f80u;
    }
    wait_vm<0>();
    wait_lgkm0();
    bar();
  }
  STAMP(1, 1);
  const int h = l >> 5, blk = (l >> 4) & 1, q = (l & 15) >> 2, p = l & 3, i32 = l & 31;
  bf16x8 q0; s16x4 q1;
  {
    const unsigned aq = sb + A_Q + (8 * h + q) * A_QS + (32 * wid + 16 * blk + 4 * p) * 2;
    q0 = cat(lds_tr(aq), lds_tr(aq + 4 * A_QS));
    u32x2 t = __builtin_bit_cast(u32x2, lds_tr(sb + A_Q + (16 + (q & 1)) * A_QS + (32 * wid + 16 * blk + 4 * p) * 2));
    t[0] = h ? 0u : t[0];
    t[1] = h ? 0u : 0x00003f80u;
    q1 = __builtin_bit_cast(s16x4, t);
  }
  const int pp = ((p & 1) << 1) | (p >> 1);
  const unsigned ak0 = sb + A_K + (8 * h + q) * A_KS + (32 * wid + 16 * blk + 4 * pp) * 2;
  const unsigned ak1 = sb + A_K + (16 + q) * A_KS + (32 * wid + 16 * blk + 4 * pp) * 2;
  auto qk = [&](int t) {
    bf16x8 k0 = cat(lds_tr(ak0 + 64 * t), lds_tr(ak0 + 64 * t + 4 * A_KS));
    s16x4 k1 = __builtin_bit_cast(s16x4, lds_tr(ak1 + 64 * t));
    f32x16 z = {};
    z = __builtin_amdgcn_mfma_f32_32x32x16_bf16(k0, q0, z, 0, 0, 0);
    return __builtin_amdgcn_mfma_f32_32x32x8bf16_1k(k1, q1, z, 0, 0, 0);
  };
  f32x16 sc[8];
  const int dd = i32 - 8 * h;
  {
    f32x16 s0 = qk(0), s8 = qk(8);
#pragma unroll
    for (int r = 0; r < 16; ++r) sc[0][r] = (16 * (r >> 3) + (r & 7) <= dd) ? s8[r] : s0[r];
  }
#pragma unroll
  for (int t = 1; t < 8; ++t) sc[t] = qk(t);
  float m = sc[0][0];
#pragma unroll
  for (int t = 0; t < 8; ++t)
#pragma unroll
    for (int r = 0; r < 16; r += 2) m = __builtin_fmaxf(__builtin_fmaxf(m, sc[t][r]), sc[t][r + 1]);
  m = __builtin_fmaxf(m, __shfl_xor(m, 32));
  int vr = i32;
  if (i32 >= 19) vr = (i32 >= 20 && i32 < 28) ? 0 : 4;
  const unsigned av = sb + A_V + vr * A_VS + (32 * wid + 8 * h) * 2;
  f32x16 o = {};
  auto pv = [&](int t, u32x4 p0, u32x4 p1) {
    o = __builtin_amdgcn_mfma_f32_32x32x16_bf16(lds_b128(av + 64 * t), __builtin_bit_cast(bf16x8, p0), o, 0, 0, 0);
    o = __builtin_amdgcn_mfma_f32_32x32x16_bf16(lds_b128(av + 64 * t + 32), __builtin_bit_cast(bf16x8, p1), o, 0, 0, 0);
  };
#pragma unroll
  for (int t = 0; t < 8; ++t) {
    unsigned pk[8];
#pragma unroll
    for (int r = 0; r < 8; ++r)
      pk[r] = pk2(__builtin_amdgcn_exp2f(sc[t][2 * r] - m), __builtin_amdgcn_exp2f(sc[t][2 * r + 1] - m));
    if (t == 0) {
      unsigned pd[8], pf[8];
#pragma unroll
      for (int r = 0; r < 8; ++r) {
        const int j = 16 * (r >> 2) + 2 * (r & 3);
        unsigned mk = (j + 1 <= dd) ? 0xffffffffu : ((j <= dd) ? 0x0000ffffu : 0u);
        pd[r] = pk[r] & mk;
        pf[r] = pk[r] ^ pd[r];
      }
      pv(0, (u32x4){pf[0], pf[1], pf[2], pf[3]}, (u32x4){pf[4], pf[5], pf[6], pf[7]});
      pv(8, (u32x4){pd[0], pd[1], pd[2], pd[3]}, (u32x4){pd[4], pd[5], pd[6], pd[7]});
    } else {
      pv(t, (u32x4){pk[0], pk[1], pk[2], pk[3]}, (u32x4){pk[4], pk[5], pk[6], pk[7]});
    }
  }
  STAMP(1, 2);
  const float ls = __shfl(o[10], i32);
  const float inv = __builtin_amdgcn_rcpf(ls);
  const int tok = 256 * tb + 32 * wid + i32;
  u32x4 om = {pk2(o[0] * inv, o[1] * inv), pk2(o[2] * inv, o[3] * inv), pk2(o[4] * inv, o[5] * inv), pk2(o[6] * inv, o[7] * inv)};
  st16_wt(ws + WS_OMAIN + ((size_t)(tok >> 4) * 16 + (head >> 1)) * 1024 + (2 * (head & 1) + h) * 256 + (tok & 15) * 16, om);
  if (h == 0) st4_wt(ws + WS_OTAIL + ((size_t)head * 2048 + tok) * 4, pk2(o[8] * inv, o[9] * inv));
  STAMP(1, 3);
}

constexpr int K3_NS = 7, K3_STAGE = 20480, K3_NST = 9;
constexpr int K3_AUX = K3_NS * K3_STAGE, K3_LDS = K3_AUX + 4 * 256;
__global__ __launch_bounds__(512, 2) void k_proj(const u16* __restrict__ x, const u16* __restrict__ wo, char* __restrict__ ws PROF_ARG) {
  extern __shared__ __attribute__((aligned(16))) char smem[];
  STAMP(2, 0);
  const int wg = blockIdx.x, tid = threadIdx.x, l = tid & 63;
  const int wid = __builtin_amdgcn_readfirstlane(tid >> 6);
  const int idx = wg >> 3;
  const int tt = (wg & 7) * 4 + (idx & 3), ct = idx >> 2;
  const unsigned sb = (unsigned)(size_t)smem;
  if (wid >= 4) {
    const int lw = wid - 4;
    unsigned vm[3], vt[3];
#pragma unroll
    for (int n = 0; n < 3; ++n) {
      const int c = 64 * (3 * lw + n) + l, R = c / 12, pos = c - 12 * R, rho = R & 31, ks = R >> 5;
      const int ch = pos ^ (((rho >> 2) & 1) << 1);
      vm[n] = (36 * ks + 18 * ((rho >> 3) & 1) + 8 * (rho >> 4) + (rho & 7)) * 1152 + ch * 16;
      const int hd = 16 * ks + 4 * ((rho >> 2) & 3) + 2 * (rho >> 4) + ((rho >> 1) & 1);
      vt[n] = (18 * hd + 16 + (rho & 1)) * 1152 + ch * 16;
    }
    const char* bw = (const char*)wo + ct * 192;
    const char* bo = ws + WS_OMAIN + (size_t)(4 * tt + lw) * 16384;
    const char* bt = ws + WS_OTAIL + tt * 256;
    auto issue = [&](int s, int sl) {
      const unsigned slot = sb + sl * K3_STAGE;
      if (s < 8) {
        glds<0>(bo + s * 2048, l * 16, slot + lw * 2048);
        glds<1024>(bo + s * 2048, l * 16, slot + lw * 2048);
#pragma unroll
        for (int n = 0; n < 3; ++n) glds<0>(bw + s * 72 * 1152, vm[n], slot + 8192 + (3 * lw + n) * 1024);
      } else {
        const unsigned v = (8 * lw + (l >> 4)) * 8192 + (l & 15) * 16;
        glds<0>(bt, v, slot + lw * 2048);
        glds<0>(bt + 4 * 8192, v, slot + lw * 2048 + 1024);
#pragma unroll
        for (int n = 0; n < 3; ++n) glds<0>(bw, vt[n], slot + 8192 + (3 * lw + n) * 1024);
      }
    };
    for (int s = 0; s < 6; ++s) issue(s, s);
    wait_vm<25>(); bar();
    issue(6, 6); wait_vm<25>(); bar();
    issue(7, 0); wait_vm<25>(); bar();
    issue(8, 1); wait_vm<25>(); bar();
    wait_vm<20>(); bar();
    wait_vm<15>(); bar();
    wait_vm<10>(); bar();
    wait_vm<5>(); bar();
    wait_vm<0>(); bar();
    return;
  }
  const int mh = wid >> 1, nh = wid & 1, i16 = l & 15, g = l >> 4, q = (l & 15) >> 2, p = l & 3;
  bf16x8 xr[2][2];
#pragma unroll
  for (int m = 0; m < 2; ++m)
#pragma unroll
    for (int w = 0; w < 2; ++w)
      xr[m][w] = *(const bf16x8*)(x + (size_t)(64 * tt + 32 * mh + 16 * m + i16) * 576 + 96 * ct + 48 * nh + 16 * w + 8 * g);
  unsigned aw[3];
#pragma unroll
  for (int c = 0; c < 3; ++c) aw[c] = 8192 + (4 * g + q) * 192 + (((3 * nh + c) ^ (g & 1)) * 32) + 8 * p;
  const unsigned ao = (2 * mh) * 2048 + l * 16;
  f32x4 acc[2][3];
  {
    u32x4 s0, s1;
#pragma unroll
    for (int e = 0; e < 4; ++e) {
      const int k = 8 * g + 2 * e;
      s0[e] = (k == i16 ? 0x3f80u : 0u) | (k + 1 == i16 ? 0x3f800000u : 0u);
      s1[e] = (k == i16 + 16 ? 0x3f80u : 0u) | (k + 1 == i16 + 16 ? 0x3f800000u : 0u);
    }
    const bf16x8 lo = __builtin_bit_cast(bf16x8, s0), hi = __builtin_bit_cast(bf16x8, s1);
    const f32x4 z = {0, 0, 0, 0};
#pragma unroll
    for (int m = 0; m < 2; ++m) {
      acc[m][0] = mfma16(xr[m][0], lo, z);
      acc[m][1] = mfma16(xr[m][0], hi, z);
      acc[m][2] = mfma16(xr[m][1], hi, z);
    }
  }
  bar();
  STAMP(2, 1);
  int sl = 0;
  for (int s = 0; s < 8; ++s) {
    const unsigned slot = sb + sl * K3_STAGE;
    bf16x8 oa[2][2], wb[2][3];
#pragma unroll
    for (int ks = 0; ks < 2; ++ks) {
#pragma unroll
      for (int m = 0; m < 2; ++m) oa[ks][m] = lds_b128(slot + ao + m * 2048 + ks * 1024);
#pragma unroll
      for (int c = 0; c < 3; ++c) wb[ks][c] = cat(lds_tr(slot + aw[c] + ks * 6144), lds_tr(slot + aw[c] + ks * 6144 + 3072));
    }
#pragma unroll
    for (int ks = 0; ks < 2; ++ks)
#pragma unroll
      for (int m = 0; m < 2; ++m)
#pragma unroll
        for (int c = 0; c < 3; ++c) acc[m][c] = mfma16(oa[ks][m], wb[ks][c], acc[m][c]);
    wait_lgkm0(); bar();
    sl = sl == K3_NS - 1 ? 0 : sl + 1;
  }
  {
    const unsigned slot = sb + 1 * K3_STAGE;
#pragma unroll
    for (int ks = 0; ks < 2; ++ks) {
      bf16x8 oa[2], wb[3];
#pragma unroll
      for (int m = 0; m < 2; ++m) {
        u32x4 t;
#pragma unroll
        for (int e = 0; e < 4; ++e) t[e] = *(const LDS3 unsigned*)(slot + (16 * ks + 4 * g + e) * 256 + (32 * mh + 16 * m + i16) * 4);
        oa[m] = __builtin_bit_cast(bf16x8, t);
      }
#pragma unroll
      for (int c = 0; c < 3; ++c) wb[c] = cat(lds_tr(slot + aw[c] + ks * 6144), lds_tr(slot + aw[c] + ks * 6144 + 3072));
#pragma unroll
      for (int m = 0; m < 2; ++m)
#pragma unroll
        for (int c = 0; c < 3; ++c) acc[m][c] = mfma16(oa[m], wb[c], acc[m][c]);
    }
  }
  STAMP(2, 2);
  u64* stats = (u64*)(ws + WS_STATS) + (size_t)(2 * tt + mh) * 12 * 32;
  {
    float mysum = 0, mym2 = 0;
#pragma unroll
    for (int m = 0; m < 2; ++m)
#pragma unroll
      for (int j = 0; j < 4; ++j) {
        const float s = row16_sum(acc[m][0][j] + acc[m][1][j] + acc[m][2][j]);
        const float mu = s * (1.f / 48.f);
        const float a = acc[m][0][j] - mu, b = acc[m][1][j] - mu, c = acc[m][2][j] - mu;
        const float m2 = row16_sum(a * a + b * b + c * c);
        if (i16 == 4 * m + j) { mysum = s; mym2 = m2; }
      }
    if (i16 < 8) {
      const int row = 16 * (i16 >> 2) + 4 * g + (i16 & 3);
      st8_wt(stats + (2 * ct + nh) * 32 + row, (u32x2){__float_as_uint(mysum), __float_as_uint(mym2) | 0x80000000u});
    }
  }
  const size_t fo = ((size_t)(2 * tt + mh) * 36 + 6 * ct + 3 * nh) * 1024 + l * 16;
#pragma unroll
  for (int c = 0; c < 3; ++c) {
    u32x4 hv = {pk2(acc[0][c][0], acc[0][c][1]), pk2(acc[0][c][2], acc[0][c][3]), pk2(acc[1][c][0], acc[1][c][1]), pk2(acc[1][c][2], acc[1][c][3])};
    st16_wt(ws + WS_HT + fo + c * 1024, hv);
  }
  float ps[6], pm[6];
  {
    const u64* src = stats + (6 * (l >> 5)) * 32 + (l & 31);
    for (int spin = 0; spin < (1 << 20); ++spin) {
      bool ok = true;
#pragma unroll
      for (int k = 0; k < 6; ++k) {
        const u64 v = __hip_atomic_load(src + k * 32, __ATOMIC_RELAXED, __HIP_MEMORY_SCOPE_AGENT);
        ps[k] = __uint_as_float((unsigned)v);
        pm[k] = __uint_as_float((unsigned)(v >> 32) & 0x7fffffffu);
        ok &= (v >> 63) != 0;
      }
      if (__all(ok)) break;
      __builtin_amdgcn_s_sleep(1);
    }
  }
  STAMP(2, 3);
  {
    float s = ((ps[0] + ps[1]) + (ps[2] + ps[3])) + (ps[4] + ps[5]);
    s += __shfl_xor(s, 32);
    const float mu = s * (1.f / 576.f);
    float m2 = 0;
#pragma unroll
    for (int k = 0; k < 6; ++k) { const float d = ps[k] * (1.f / 48.f) - mu; m2 += pm[k] + 48.f * d * d; }
    m2 += __shfl_xor(m2, 32);
    const float var = m2 * (1.f / 576.f);
    const float r1 = __builtin_amdgcn_rsqf(var + 1e-5f);
    const float r2 = __builtin_amdgcn_rsqf(var * r1 * r1 + 1e-5f);
    const unsigned a = sb + K3_AUX + wid * 256;
    if (l < 32) *(LDS3 f32x2*)(a + l * 8) = (f32x2){mu, r1 * r2};
    wait_lgkm0();
    f32x4 st[2][2];
#pragma unroll
    for (int m = 0; m < 2; ++m)
#pragma unroll
      for (int k = 0; k < 2; ++k) st[m][k] = *(const LDS3 f32x4*)(a + (16 * m + 4 * g + 2 * k) * 8);
#pragma unroll
    for (int c = 0; c < 3; ++c) {
      float v[8];
#pragma unroll
      for (int e = 0; e < 8; ++e) v[e] = (acc[e >> 2][c][e & 3] - st[e >> 2][(e >> 1) & 1][(e & 1) * 2]) * st[e >> 2][(e >> 1) & 1][(e & 1) * 2 + 1];
      u32x4 hv = {pk2(v[0], v[1]), pk2(v[2], v[3]), pk2(v[4], v[5]), pk2(v[6], v[7])};
      st16_wt(ws + WS_HNT + fo + c * 1024, hv);
    }
  }
  STAMP(2, 4);
}

constexpr int G1_NS = 5, G1_STAGE = 28672, G1_NST = 32, G1_LDS = G1_NS * G1_STAGE;
DEV float gelu_t(float u) {
  const float k1 = -2.f * 0.7978845608028654f * 1.4426950408889634f, k2 = k1 * 0.044715f;
  const float e = __builtin_amdgcn_exp2f(u * (k1 + k2 * u * u));
  return u * __builtin_amdgcn_rcpf(1.f + e);
}
__global__ __launch_bounds__(512, 2) void k_mlp1(const u16* __restrict__ w1, const u16* __restrict__ b1, char* __restrict__ ws PROF_ARG) {
  extern __shared__ __attribute__((aligned(16))) char smem[];
  STAMP(3, 0);
  const int wg = blockIdx.x, tid = threadIdx.x, l = tid & 63;
  const int wid = __builtin_amdgcn_readfirstlane(tid >> 6);
  const int tile = (wg & 7) * 27 + (wg >> 3), ft = tile / 6, dt = tile - 6 * ft;
  const unsigned sb = (unsigned)(size_t)smem;
  if (wid >= 4) {
    const int lw = wid - 4;
    const int f = 4 * (lw & 1) + (l >> 4);
    const unsigned vb = (4 * lw + (l >> 4)) * 9216 + (((l & 15) ^ (f << 1)) * 16);
    const char* pb = (const char*)w1 + ft * 256;
    const char* pa = ws + WS_HNT + (size_t)((lw >> 1) * 36 + 6 * dt + 3 * (lw & 1)) * 1024;
    unsigned slot = sb;
    auto issue = [&]() {
      glds<0>(pa, l * 16, slot + lw * 3072);
      glds<1024>(pa, l * 16, slot + lw * 3072);
      glds<2048>(pa, l * 16, slot + lw * 3072);
#pragma unroll
      for (int i = 0; i < 4; ++i) glds<0>(pb + i * 16 * 9216, vb, slot + 12288 + (lw + 4 * i) * 1024);
      pa += 72 * 1024; pb += 64 * 9216;
      slot = slot == sb + (G1_NS - 1) * G1_STAGE ? sb : slot + G1_STAGE;
    };
    for (int s = 0; s < G1_NS - 1; ++s) issue();
    wait_vm<21>(); bar();
    for (int s = 0; s < G1_NST - (G1_NS - 1); ++s) { issue(); wait_vm<21>(); bar(); }
    wait_vm<14>(); bar();
    wait_vm<7>(); bar();
    wait_vm<0>(); bar();
    return;
  }
  const int mh = wid >> 1, nh = wid & 1, g = l >> 4, q = (l & 15) >> 2, p = l & 3;
  u32x2 bias[4];
#pragma unroll
  for (int c = 0; c < 4; ++c) bias[c] = *(const u32x2*)(b1 + 128 * ft + 64 * nh + 16 * c + 4 * g);
  const unsigned aa = 3 * mh * 1024 + l * 16;
  unsigned aw[4];
  const int f = 4 * (g & 1) + q;
#pragma unroll
  for (int c = 0; c < 4; ++c) aw[c] = 12288 + (4 * g + q) * 256 + (((4 * nh + c) ^ f) * 32) + 8 * p;
  f32x4 acc[4][3];
#pragma unroll
  for (int c = 0; c < 4; ++c)
#pragma unroll
    for (int u = 0; u < 3; ++u) acc[c][u] = (f32x4){0, 0, 0, 0};
  bf16x8 ha[2][3], wb[2][4];
  auto rd = [&](int ks, unsigned slot) {
#pragma unroll
    for (int u = 0; u < 3; ++u) ha[ks][u] = lds_b128(slot + aa + ks * 6144 + u * 1024);
#pragma unroll
    for (int c = 0; c < 4; ++c) wb[ks][c] = cat(lds_tr(slot + aw[c] + ks * 8192), lds_tr(slot + aw[c] + ks * 8192 + 4096));
  };
  auto mm = [&](int ks) {
#pragma unroll
    for (int c = 0; c < 4; ++c)
#pragma unroll
      for (int u = 0; u < 3; ++u) acc[c][u] = mfma16(wb[ks][c], ha[ks][u], acc[c][u]);
  };
  auto ilv = [&]() {
#pragma unroll
    for (int k = 0; k < 11; ++k) { __builtin_amdgcn_sched_group_barrier(0x8, 1, 0); __builtin_amdgcn_sched_group_barrier(0x100, 1, 0); }
    __builtin_amdgcn_sched_group_barrier(0x8, 1, 0);
  };
  bar();
  STAMP(3, 1);
  rd(0, sb); rd(1, sb); mm(0);
  unsigned slot = sb + G1_STAGE;
  for (int s = 1; s < G1_NST; ++s) {
    wait_lgkm0(); bar();
    __builtin_amdgcn_sched_barrier(0);
    rd(0, slot); mm(1); ilv();
    __builtin_amdgcn_sched_barrier(0);
    rd(1, slot); mm(0); ilv();
    __builtin_amdgcn_sched_barrier(0);
    slot = slot == sb + (G1_NS - 1) * G1_STAGE ? sb : slot + G1_STAGE;
  }
  mm(1);
  STAMP(3, 2);
#pragma unroll
  for (int a = 0; a < 2; ++a)
#pragma unroll
    for (int u = 0; u < 3; ++u) {
      float v[8];
#pragma unroll
      for (int e = 0; e < 8; ++e) {
        const unsigned bw = bias[2 * a + (e >> 2)][(e >> 1) & 1];
        v[e] = gelu_t(acc[2 * a + (e >> 2)][u][e & 3] + ((e & 1) ? bfhi(bw) : bflo(bw)));
      }
      u32x4 o = {pk2(v[0], v[1]), pk2(v[2], v[3]), pk2(v[4], v[5]), pk2(v[6], v[7])};
      st16_wt(ws + WS_UT + ((size_t)(4 * ft + 2 * nh + a) * 36 + 6 * dt + 3 * mh + u) * 1024 + l * 16, o);
    }
  STAMP(3, 3);
}

constexpr int G2_NS = 4, G2_STAGE = 34816, G2_NST = 18, G2_LDS = G2_NS * G2_STAGE;
template <int NA>
DEV void g2_loader(const char* pa, const char* pb, unsigned vb, unsigned sb, unsigned aoff, int lw, int l) {
  int sl = 0;
  auto issue = [&]() {
    const unsigned slot = sb + sl * G2_STAGE;
    glds<0>(pa, l * 16, slot + aoff);
    glds<1024>(pa, l * 16, slot + aoff);
    glds<2048>(pa, l * 16, slot + aoff);
    glds<3072>(pa, l * 16, slot + aoff);
    if (NA == 5) glds<0>(pa + 4096, l * 16, slot + aoff + 4096);
#pragma unroll
    for (int i = 0; i < 4; ++i) glds<0>(pb + i * 16 * 4096, vb, slot + 18432 + (lw + 4 * i) * 1024);
    pa += 72 * 1024; pb += 64 * 4096;
    sl = (sl + 1) & (G2_NS - 1);
  };
  for (int s = 0; s < G2_NS - 1; ++s) issue();
  wait_vm<2 * (NA + 4)>(); bar();
  for (int s = 0; s < G2_NST - (G2_NS - 1); ++s) { issue(); wait_vm<2 * (NA + 4)>(); bar(); }
  wait_vm<NA + 4>(); bar();
  wait_vm<0>(); bar();
}
__global__ __launch_bounds__(512, 2) void k_mlp2(const u16* __restrict__ w2, const u16* __restrict__ b2, char* __restrict__ ws, u16* __restrict__ out PROF_ARG) {
  extern __shared__ __attribute__((aligned(16))) char smem[];
  STAMP(4, 0);
  const int wg = blockIdx.x, tid = threadIdx.x, l = tid & 63;
  const int wid = __builtin_amdgcn_readfirstlane(tid >> 6);
  const int xcd = wg & 7, idx = wg >> 3;
  const int kq = xcd >> 1, st = (xcd & 1) * 8 + (idx >> 2), dt = idx & 3;
  const int tile = st * 4 + dt;
  const unsigned sb = (unsigned)(size_t)smem;
  const int g = l >> 4, q = (l & 15) >> 2, p = l & 3;
  char* part = ws + WS_PART + (size_t)tile * 16 * 9216;
  if (wid >= 4) {
    const int lw = wid - 4;
    const int f = 4 * (lw & 1) + (l >> 4);
    const unsigned vb = (4 * lw + (l >> 4)) * 4096 + (((l & 15) ^ (f << 1)) * 16);
    const char* pb = (const char*)w2 + (size_t)kq * 1152 * 4096 + st * 256;
    const int ks = lw & 1, u0 = (lw >> 1) * 5;
    const char* pa = ws + WS_UT + (size_t)((36 * kq + ks) * 36 + 9 * dt + u0) * 1024;
    const unsigned aoff = (9 * ks + u0) * 1024;
    if (lw < 2) g2_loader<5>(pa, pb, vb, sb, aoff, lw, l); else g2_loader<4>(pa, pb, vb, sb, aoff, lw, l);
  } else {
    const unsigned aa = l * 16;
    unsigned aw[2];
    const int f = 4 * (g & 1) + q;
#pragma unroll
    for (int c = 0; c < 2; ++c) aw[c] = 18432 + (4 * g + q) * 256 + (((2 * wid + c) ^ f) * 32) + 8 * p;
    f32x4 acc[2][9];
#pragma unroll
    for (int c = 0; c < 2; ++c)
#pragma unroll
      for (int u = 0; u < 9; ++u) acc[c][u] = (f32x4){0, 0, 0, 0};
    bf16x8 ua[2][9], wb[2][2];
    auto rd = [&](int ks, unsigned slot) {
#pragma unroll
      for (int c = 0; c < 2; ++c) wb[ks][c] = cat(lds_tr(slot + aw[c] + ks * 8192), lds_tr(slot + aw[c] + ks * 8192 + 4096));
#pragma unroll
      for (int u = 0; u < 9; ++u) ua[ks][u] = lds_b128(slot + aa + ks * 9216 + u * 1024);
    };
    auto mm = [&](int ks) {
#pragma unroll
      for (int u = 0; u < 9; ++u)
#pragma unroll
        for (int c = 0; c < 2; ++c) acc[c][u] = mfma16(wb[ks][c], ua[ks][u], acc[c][u]);
    };
    auto ilv = [&]() {
#pragma unroll
      for (int k = 0; k < 13; ++k) { __builtin_amdgcn_sched_group_barrier(0x8, 1, 0); __builtin_amdgcn_sched_group_barrier(0x100, 1, 0); }
      __builtin_amdgcn_sched_group_barrier(0x8, 5, 0);
    };
    bar();
    STAMP(4, 1);
    rd(0, sb); rd(1, sb); mm(0);
    for (int s = 1; s < G2_NST; ++s) {
      const unsigned slot = sb + (s & (G2_NS - 1)) * G2_STAGE;
      wait_lgkm0(); bar();
      __builtin_amdgcn_sched_barrier(0);
      rd(0, slot); mm(1); ilv();
      __builtin_amdgcn_sched_barrier(0);
      rd(1, slot); mm(0); ilv();
      __builtin_amdgcn_sched_barrier(0);
    }
    mm(1);
    STAMP(4, 2);
    if (wid == kq) {
#pragma unroll
      for (int u = 0; u < 9; ++u) {
        *(LDS3 f32x4*)(sb + 2 * G2_STAGE + u * 2048 + l * 16) = acc[0][u];
        *(LDS3 f32x4*)(sb + 2 * G2_STAGE + u * 2048 + 1024 + l * 16) = acc[1][u];
      }
    } else {
#pragma unroll
      for (int u = 0; u < 9; ++u) {
        u32x4 o = {pk2(acc[0][u][0], acc[0][u][1]), pk2(acc[0][u][2], acc[0][u][3]), pk2(acc[1][u][0], acc[1][u][1]), pk2(acc[1][u][2], acc[1][u][3])};
        st16_wt(part + (wid * 4 + kq) * 9216 + u * 1024 + l * 16, o);
      }
      wait_vm<0>();
      if (l == 0) __hip_atomic_store((unsigned*)(ws + WS_FLAGS + tile * 1024 + (wid * 4 + kq) * 64), 1u, __ATOMIC_RELAXED, __HIP_MEMORY_SCOPE_AGENT);
    }
  }
  const int sstep = 4 * st + kq;
  const u32x2 bb0 = *(const u32x2*)(b2 + 32 * sstep + 4 * g), bb1 = *(const u32x2*)(b2 + 32 * sstep + 16 + 4 * g);
  const char* hT = ws + WS_HT + ((size_t)sstep * 36 + 9 * dt) * 1024 + l * 16;
  u32x4 hv0 = *(const u32x4*)(hT + wid * 1024), hv1 = *(const u32x4*)(hT + 8 * 1024);
  if (wid == 7) {
    const unsigned* fl = (const unsigned*)(ws + WS_FLAGS + tile * 1024 + (kq * 4 + (l & 3)) * 64);
    for (int spin = 0; spin < (1 << 20); ++spin) {
      const unsigned v = __hip_atomic_load(fl, __ATOMIC_RELAXED, __HIP_MEMORY_SCOPE_AGENT);
      if (__all(v == 1u || (l & 3) == kq)) break;
      __builtin_amdgcn_s_sleep(1);
    }
  }
  wait_lgkm0(); bar();
  STAMP(4, 3);
  const unsigned tb_ = sb + 3 * G2_STAGE;
  auto fin = [&](int u, u32x4 hv) {
    u32x4 pv[3];
#pragma unroll
    for (int k = 0; k < 3; ++k) {
      const int src = k + (k >= kq ? 1 : 0);
      const char* a = part + (kq * 4 + src) * 9216 + u * 1024 + l * 16;
      asm volatile("global_load_dwordx4 %0, %1, off sc1" : "=&v"(pv[k]) : "v"(a) : "memory");
    }
    const f32x4 a0 = *(const LDS3 f32x4*)(sb + 2 * G2_STAGE + u * 2048 + l * 16);
    const f32x4 a1 = *(const LDS3 f32x4*)(sb + 2 * G2_STAGE + u * 2048 + 1024 + l * 16);
    asm volatile("s_waitcnt vmcnt(0)" : "+v"(pv[0]), "+v"(pv[1]), "+v"(pv[2])::"memory");
    float v[8];
#pragma unroll
    for (int e = 0; e < 8; ++e) {
      const int w = e >> 1;
      const unsigned bw = e < 4 ? bb0[w] : bb1[w - 2];
      float t = e < 4 ? a0[e] : a1[e - 4];
      if (e & 1) t += bfhi(pv[0][w]) + bfhi(pv[1][w]) + bfhi(pv[2][w]) + bfhi(bw) + bfhi(hv[w]);
      else t += bflo(pv[0][w]) + bflo(pv[1][w]) + bflo(pv[2][w]) + bflo(bw) + bflo(hv[w]);
      v[e] = t;
    }
    const unsigned a = tb_ + (16 * u + (l & 15)) * 72 + 8 * g;
    *(LDS3 u32x2*)(a) = (u32x2){pk2(v[0], v[1]), pk2(v[2], v[3])};
    *(LDS3 u32x2*)(a + 32) = (u32x2){pk2(v[4], v[5]), pk2(v[6], v[7])};
    wait_lgkm0();
    const unsigned r = tb_ + (16 * u + 8 * (g >> 1) + q) * 72 + 32 * (g & 1) + 8 * p;
    const bf16x8 o = cat(lds_tr(r), lds_tr(r + 4 * 72));
    *(bf16x8*)(out + (size_t)(32 * sstep + 16 * (g & 1) + (l & 15)) * 576 + 144 * dt + 16 * u + 8 * (g >> 1)) = o;
  };
  fin(wid, hv0);
  if (wid == 4) fin(8, hv1);
  STAMP(4, 4);
}

extern "C" void kernel_launch(void* const* d_in, const int* in_sizes, int n_in, void* d_out, int out_size, void* d_ws, size_t ws_size,
                              hipStream_t stream) {
  const u16* x = (const u16*)d_in[0];
  const u16* wqkv = (const u16*)d_in[1];
  const u16* wo = (const u16*)d_in[2];
  const u16* w1 = (const u16*)d_in[4];
  const u16* b1 = (const u16*)d_in[5];
  const u16* w2 = (const u16*)d_in[6];
  const u16* b2 = (const u16*)d_in[7];
  char* ws = (char*)d_ws;
  (void)hipFuncSetAttribute((const void*)k_qkv, hipFuncAttributeMaxDynamicSharedMemorySize, K1_LDS);
  (void)hipFuncSetAttribute((const void*)k_proj, hipFuncAttributeMaxDynamicSharedMemorySize, K3_LDS);
  (void)hipFuncSetAttribute((const void*)k_mlp1, hipFuncAttributeMaxDynamicSharedMemorySize, G1_LDS);
  (void)hipFuncSetAttribute((const void*)k_mlp2, hipFuncAttributeMaxDynamicSharedMemorySize, G2_LDS);
  k_qkv<<<256, 512, K1_LDS, stream>>>(x, wqkv, ws PROF_PASS);
  k_attn<<<256, 512, A_LDS, stream>>>(ws PROF_PASS);
  k_proj<<<192, 512, K3_LDS, stream>>>(x, wo, ws PROF_PASS);
  k_mlp1<<<216, 512, G1_LDS, stream>>>(w1, b1, ws PROF_PASS);
  k_mlp2<<<256, 512, G2_LDS, stream>>>(w2, b2, ws, (u16*)d_out PROF_PASS);
}
```

```cpp
#include <hip/hip_runtime.h>
#include <hip/hip_bf16.h>
#include <stdint.h>

typedef unsigned short u16;
typedef unsigned long long u64;
typedef __attribute__((ext_vector_type(8))) __bf16 bf16x8;
typedef __attribute__((ext_vector_type(4))) __bf16 bf16x4;
typedef __attribute__((ext_vector_type(2))) __bf16 bf16x2;
typedef __attribute__((ext_vector_type(4))) short s16x4;
typedef __attribute__((ext_vector_type(4))) float f32x4;
typedef __attribute__((ext_vector_type(2))) float f32x2;
typedef __attribute__((ext_vector_type(16))) float f32x16;
typedef __attribute__((ext_vector_type(4))) unsigned u32x4;
typedef __attribute__((ext_vector_type(2))) unsigned u32x2;
#define LDS3 __attribute__((address_space(3)))
#define DEV __device__ __forceinline__

constexpr int S_ = 2048, D_ = 576, F_ = 4608;
constexpr size_t WS_FLAGS = 0;
constexpr size_t WS_STATS = 65536;
constexpr size_t WS_ZERO_BYTES = 65536 + 196608;
constexpr size_t WS_QKVT = 262144;
constexpr size_t WS_OMAIN = WS_QKVT + (size_t)1728 * 2048 * 2;
constexpr size_t WS_OTAIL = WS_OMAIN + (size_t)128 * 16 * 1024;
constexpr size_t WS_HT = WS_OTAIL + (size_t)32 * 2048 * 4;
constexpr size_t WS_HNT = WS_HT + (size_t)64 * 36 * 1024;
constexpr size_t WS_UT = WS_HNT + (size_t)64 * 36 * 1024;
constexpr size_t WS_PART = WS_UT + (size_t)144 * 36 * 1024;
constexpr size_t WS_END = WS_PART + (size_t)64 * 16 * 9 * 1024;

#define STAMP(ph, i)
#define PROF_ARG
#define PROF_PASS

template <int OFF>
DEV void glds(const void* sbase, unsigned voff, unsigned lds) {
  asm volatile("s_mov_b32 m0, %2\n\ts_nop 0\n\tglobal_load_lds_dwordx4 %0, %1 offset:%3" ::"v"(voff), "s"(sbase), "s"(lds), "i"(OFF) : "memory");
}
template <int OFF>
DEV void glds_nt(const void* sbase, unsigned voff, unsigned lds) {
  asm volatile("s_mov_b32 m0, %2\n\ts_nop 0\n\tglobal_load_lds_dwordx4 %0, %1 offset:%3 nt" ::"v"(voff), "s"(sbase), "s"(lds), "i"(OFF) : "memory");
}
template <int N> DEV void wait_vm() { asm volatile("s_waitcnt vmcnt(%0)" ::"i"(N) : "memory"); }
DEV void wait_lgkm0() { asm volatile("s_waitcnt lgkmcnt(0)" ::: "memory"); }
DEV void bar() { asm volatile("" ::: "memory"); __builtin_amdgcn_s_barrier(); asm volatile("" ::: "memory"); }
DEV bf16x8 lds_b128(unsigned a) { return *(const LDS3 bf16x8*)a; }
DEV bf16x4 lds_tr(unsigned a) { return __builtin_amdgcn_ds_read_tr16_b64_v4bf16((LDS3 bf16x4*)a); }
DEV bf16x8 cat(bf16x4 a, bf16x4 b) { return __builtin_shufflevector(a, b, 0, 1, 2, 3, 4, 5, 6, 7); }
DEV f32x4 mfma16(bf16x8 a, bf16x8 b, f32x4 c) { return __builtin_amdgcn_mfma_f32_16x16x32_bf16(a, b, c, 0, 0, 0); }
DEV unsigned pk2(float a, float b) { bf16x2 p = {(__bf16)a, (__bf16)b}; return __builtin_bit_cast(unsigned, p); }
DEV float bflo(unsigned u) { return __uint_as_float(u << 16); }
DEV float bfhi(unsigned u) { return __uint_as_float(u & 0xffff0000u); }
DEV void st16_wt(void* p, u32x4 v) { asm volatile("global_store_dwordx4 %0, %1, off sc1\n\ts_nop 1" ::"v"(p), "v"(v) : "memory"); }
DEV void st8_wt(void* p, u32x2 v) { asm volatile("global_store_dwordx2 %0, %1, off sc1\n\ts_nop 1" ::"v"(p), "v"(v) : "memory"); }
DEV void st4_wt(void* p, unsigned v) { asm volatile("global_store_dword %0, %1, off sc1\n\ts_nop 1" ::"v"(p), "v"(v) : "memory"); }
template <int CTRL> DEV float dpp_add(float v) {
  return v + __builtin_bit_cast(float, __builtin_amdgcn_update_dpp(0, __builtin_bit_cast(int, v), CTRL, 0xf, 0xf, true));
}
DEV float row16_sum(float v) { v = dpp_add<0x128>(v); v = dpp_add<0x124>(v); v = dpp_add<0x122>(v); v = dpp_add<0x121>(v); return v; }
DEV float dot2(unsigned a, unsigned b, float c) {
  return __builtin_amdgcn_fdot2_f32_bf16(__builtin_bit_cast(bf16x2, a), __builtin_bit_cast(bf16x2, b), c, false);
}

constexpr int K1_NS = 4, K1_STAGE = 32768, K1_NST = 9;
constexpr int K1_AUX = K1_NS * K1_STAGE;
constexpr int K1_LDS = K1_AUX + 1024 + 16 * 512;
__global__ __launch_bounds__(512, 2) void k_qkv(const u16* __restrict__ x, const u16* __restrict__ wqkv, char* __restrict__ ws PROF_ARG) {
  extern __shared__ __attribute__((aligned(16))) char smem[];
  STAMP(0, 0);
  const int wg = blockIdx.x, tid = threadIdx.x, l = tid & 63;
  const int wid = __builtin_amdgcn_readfirstlane(tid >> 6);
  if (wg >= 224) {
    u32x4 z = {0, 0, 0, 0};
    for (unsigned i = (wg - 224) * 512 + tid; i < WS_ZERO_BYTES / 16; i += 32 * 512) ((u32x4*)ws)[i] = z;
    return;
  }
  const int xcd = wg & 7, idx = wg >> 3;
  const int tb = (xcd & 3) * 4 + (idx & 3), ct = (xcd >> 2) * 7 + (idx >> 2);
  const unsigned sb = (unsigned)(size_t)smem;
  if (wid >= 4) {
    const int lw = wid - 4;
    const unsigned vx = (8 * lw + (l >> 3)) * 1152 + (((l & 7) ^ (((l >> 4) & 1) | (lw << 1))) * 16);
    const int fw = (l >> 4) | (((lw >> 1) & 1) << 2);
    int cw = (l & 15) ^ (fw << 1);
    const int cw_real = cw;
    if (ct == 13) cw &= 7;
    const unsigned vw = (4 * lw + (l >> 4)) * 3456 + cw * 16;
    const char* bx = (const char*)x + (size_t)tb * 128 * 1152;
    const char* bw = (const char*)wqkv + ct * 256;
    auto issue = [&](int s) {
      const unsigned slot = sb + (s & (K1_NS - 1)) * K1_STAGE + lw * 1024;
      const char* px = bx + s * 128;
      const char* pw = bw + (size_t)s * 64 * 3456;
#pragma unroll
      for (int i = 0; i < 4; ++i) glds<0>(px + i * 32 * 1152, vx, slot + i * 4096);
#pragma unroll
      for (int i = 0; i < 4; ++i) glds<0>(pw + i * 16 * 3456, vw, slot + 16384 + i * 4096);
    };
    issue(0); issue(1); issue(2);
    float sx[4] = {0, 0, 0, 0}, sq[4] = {0, 0, 0, 0}, cs[8] = {0, 0, 0, 0, 0, 0, 0, 0};
    unsigned klo = 0x00003f80u, khi = 0x3f800000u;
    asm volatile("" : "+s"(klo), "+s"(khi));
    wait_vm<16>();
    bar();
    for (int s = 0; s < K1_NST; ++s) {
      if (s + 3 < K1_NST) issue(s + 3);
      const unsigned slot = sb + (s & (K1_NS - 1)) * K1_STAGE + lw * 1024 + l * 16;
#pragma unroll
      for (int i = 0; i < 4; ++i) {
        u32x4 v = *(const LDS3 u32x4*)(slot + i * 4096);
        u32x4 w = *(const LDS3 u32x4*)(slot + 16384 + i * 4096);
#pragma unroll
        for (int e = 0; e < 4; ++e) {
          sx[i] = dot2(v[e], 0x3f803f80u, sx[i]);
          sq[i] = dot2(v[e], v[e], sq[i]);
          cs[2 * e] = dot2(w[e], klo, cs[2 * e]);
          cs[2 * e + 1] = dot2(w[e], khi, cs[2 * e + 1]);
        }
      }
      if (s == K1_NST - 1) {
#pragma unroll
        for (int i = 0; i < 4; ++i) {
          float a = sx[i], b = sq[i];
          a = dpp_add<0xb1>(a); a = dpp_add<0x4e>(a); a = dpp_add<0x141>(a);
          b = dpp_add<0xb1>(b); b = dpp_add<0x4e>(b); b = dpp_add<0x141>(b);
          float mu = a * (1.f / 576.f);
          float var = fmaxf(b * (1.f / 576.f) - mu * mu, 0.f);
          float r = __builtin_amdgcn_rsqf(var + 1e-5f);
          if ((l & 7) == 0) *(LDS3 f32x2*)(sb + K1_AUX + (32 * i + 8 * lw + (l >> 3)) * 8) = (f32x2){mu, r};
        }
        const unsigned ca = sb + K1_AUX + 1024 + (lw * 4 + (l >> 4)) * 512 + cw_real * 32;
        *(LDS3 f32x4*)(ca) = (f32x4){cs[0], cs[1], cs[2], cs[3]};
        *(LDS3 f32x4*)(ca + 16) = (f32x4){cs[4], cs[5], cs[6], cs[7]};
      }
      if (s + 3 < K1_NST) wait_vm<16>(); else if (s + 2 < K1_NST) wait_vm<8>(); else wait_vm<0>();
      wait_lgkm0();
      bar();
    }
    return;
  }
  const int mh = wid >> 1, nh = wid & 1, i16 = l & 15, g = l >> 4, q = (l & 15) >> 2, p = l & 3;
  unsigned ax[2];
#pragma unroll
  for (int ks = 0; ks < 2; ++ks) ax[ks] = (64 * mh + 8 * (i16 >> 2) + (i16 & 3)) * 128 + (((4 * ks + g) ^ (i16 >> 1)) * 16);
  unsigned aw[4];
  const int f = q | ((g & 1) << 2);
#pragma unroll
  for (int c = 0; c < 4; ++c) aw[c] = 16384 + (8 * g + q) * 256 + (((4 * nh + c) ^ f) * 32) + 8 * p;
  f32x4 acc[4][4];
#pragma unroll
  for (int m = 0; m < 4; ++m)
#pragma unroll
    for (int c = 0; c < 4; ++c) acc[m][c] = (f32x4){0, 0, 0, 0};
  bf16x8 xa[2][4], wb[2][4];
  auto rd = [&](int ks, unsigned slot) {
#pragma unroll
    for (int m = 0; m < 4; ++m) xa[ks][m] = lds_b128(slot + ax[ks] + (m >> 1) * 4096 + (m & 1) * 512);
#pragma unroll
    for (int c = 0; c < 4; ++c) wb[ks][c] = cat(lds_tr(slot + aw[c] + ks * 8192), lds_tr(slot + aw[c] + ks * 8192 + 1024));
  };
  auto mm = [&](int ks) {
#pragma unroll
    for (int m = 0; m < 4; ++m)
#pragma unroll
      for (int c = 0; c < 4; ++c) acc[m][c] = mfma16(xa[ks][m], wb[ks][c], acc[m][c]);
  };
  bar();
  STAMP(0, 1);
  rd(0, sb); rd(1, sb); mm(0);
  wait_lgkm0(); bar();
  for (int s = 1; s < K1_NST; ++s) {
    const unsigned slot = sb + (s & (K1_NS - 1)) * K1_STAGE;
    rd(0, slot); mm(1);
    __builtin_amdgcn_sched_barrier(0);
    rd(1, slot); mm(0);
    wait_lgkm0(); bar();
  }
  mm(1);
  STAMP(0, 2);
  if (ct == 13 && nh == 1) return;
  float csum[4];
#pragma unroll
  for (int c = 0; c < 4; ++c) {
    float t = 0;
#pragma unroll
    for (int k = 0; k < 4; ++k) t += *(const LDS3 float*)(sb + K1_AUX + 1024 + (4 * g + k) * 512 + (64 * nh + 16 * c + i16) * 4);
    t += __shfl_xor(t, 16);
    t += __shfl_xor(t, 32);
    csum[c] = t;
  }
  char* qkvT = ws + WS_QKVT;
#pragma unroll
  for (int a = 0; a < 2; ++a) {
    f32x4 st[4];
#pragma unroll
    for (int k = 0; k < 4; ++k) st[k] = *(const LDS3 f32x4*)(sb + K1_AUX + (64 * mh + 32 * a + 8 * g + 2 * k) * 8);
#pragma unroll
    for (int c = 0; c < 4; ++c) {
      const int n = 128 * ct + 64 * nh + 16 * c + i16;
      const float sc = n < 576 ? 0.23570226039551584f * 1.4426950408889634f : 1.f;
      float v[8];
#pragma unroll
      for (int e = 0; e < 8; ++e) {
        float mu = st[e >> 1][(e & 1) * 2], r = st[e >> 1][(e & 1) * 2 + 1];
        v[e] = (acc[2 * a + (e >> 2)][c][e & 3] - mu * csum[c]) * (r * sc);
      }
      u32x4 o = {pk2(v[0], v[1]), pk2(v[2], v[3]), pk2(v[4], v[5]), pk2(v[6], v[7])};
      st16_wt(qkvT + (size_t)n * 4096 + (128 * tb + 64 * mh + 32 * a + 8 * g) * 2, o);
    }
  }
  STAMP(0, 3);
}

constexpr int A_KS = 1088, A_VS = 1040, A_QS = 576;
constexpr int A_K = 0, A_V = 20 * A_KS, A_Q = A_V + 19 * A_VS, A_LDS = A_Q + 18 * A_QS;
__global__ __launch_bounds__(512, 2) void k_attn(char* __restrict__ ws PROF_ARG) {
  extern __shared__ __attribute__((aligned(16))) char smem[];
  STAMP(1, 0);
  const int wg = blockIdx.x, tid = threadIdx.x, l = tid & 63;
  const int wid = __builtin_amdgcn_readfirstlane(tid >> 6);
  const int head = (wg & 7) * 4 + ((wg >> 3) & 3), tb = wg >> 5;
  const unsigned sb = (unsigned)(size_t)smem;
  const char* qkvT = ws + WS_QKVT;
  {
    int key0 = 256 * tb - 256;
    unsigned vk = l * 16;
    if (tb == 0) { key0 = 0; vk = (l & 31) * 16; }
    const char* bk = qkvT + (size_t)(576 + 18 * head) * 4096 + key0 * 2;
    const char* bv = bk + (size_t)576 * 4096;
    const char* bq = qkvT + (size_t)(18 * head) * 4096 + tb * 512;
    for (int r = wid; r < 18; r += 8) {
      glds<0>(bk + r * 4096, vk, sb + A_K + r * A_KS);
      if (l < 32) glds<0>(bq + r * 4096, l * 16, sb + A_Q + r * A_QS);
    }
    for (int r = 7 - wid; r < 18; r += 8) glds<0>(bv + r * 4096, vk, sb + A_V + r * A_VS);
    if (tid < 256) {
      *(LDS3 unsigned*)(sb + A_K + 18 * A_KS + tid * 4) = (tb == 0 && tid < 128) ? 0xf149f149u : 0u;
      *(LDS3 unsigned*)(sb + A_K + 19 * A_KS + tid * 4) = 0u;
    } else {
      *(LDS3 unsigned*)(sb + A_V + 18 * A_VS + (tid - 256) * 4) = 0x3f803f80u;
    }
    wait_vm<0>();
    wait_lgkm0();
    bar();
  }
  STAMP(1, 1);
  const int h = l >> 5, blk = (l >> 4) & 1, q = (l & 15) >> 2, p = l & 3, i32 = l & 31;
  bf16x8 q0; s16x4 q1;
  {
    const unsigned aq = sb + A_Q + (8 * h + q) * A_QS + (32 * wid + 16 * blk + 4 * p) * 2;
    q0 = cat(lds_tr(aq), lds_tr(aq + 4 * A_QS));
    u32x2 t = __builtin_bit_cast(u32x2, lds_tr(sb + A_Q + (16 + (q & 1)) * A_QS + (32 * wid + 16 * blk + 4 * p) * 2));
    t[0] = h ? 0u : t[0];
    t[1] = h ? 0u : 0x00003f80u;
    q1 = __builtin_bit_cast(s16x4, t);
  }
  const int pp = ((p & 1) << 1) | (p >> 1);
  const unsigned ak0 = sb + A_K + (8 * h + q) * A_KS + (32 * wid + 16 * blk + 4 * pp) * 2;
  const unsigned ak1 = sb + A_K + (16 + q) * A_KS + (32 * wid + 16 * blk + 4 * pp) * 2;
  auto qk = [&](int t) {
    bf16x8 k0 = cat(lds_tr(ak0 + 64 * t), lds_tr(ak0 + 64 * t + 4 * A_KS));
    s16x4 k1 = __builtin_bit_cast(s16x4, lds_tr(ak1 + 64 * t));
    f32x16 z = {};
    z = __builtin_amdgcn_mfma_f32_32x32x16_bf16(k0, q0, z, 0, 0, 0);
    return __builtin_amdgcn_mfma_f32_32x32x8bf16_1k(k1, q1, z, 0, 0, 0);
  };
  f32x16 sc[8];
  const int dd = i32 - 8 * h;
  {
    f32x16 s0 = qk(0), s8 = qk(8);
#pragma unroll
    for (int r = 0; r < 16; ++r) sc[0][r] = (16 * (r >> 3) + (r & 7) <= dd) ? s8[r] : s0[r];
  }
#pragma unroll
  for (int t = 1; t < 8; ++t) sc[t] = qk(t);
  float m = sc[0][0];
#pragma unroll
  for (int t = 0; t < 8; ++t)
#pragma unroll
    for (int r = 0; r < 16; r += 2) m = __builtin_fmaxf(__builtin_fmaxf(m, sc[t][r]), sc[t][r + 1]);
  m = __builtin_fmaxf(m, __shfl_xor(m, 32));
  int vr = i32;
  if (i32 >= 19) vr = (i32 >= 20 && i32 < 28) ? 0 : 4;
  const unsigned av = sb + A_V + vr * A_VS + (32 * wid + 8 * h) * 2;
  f32x16 o = {};
  auto pv = [&](int t, u32x4 p0, u32x4 p1) {
    o = __builtin_amdgcn_mfma_f32_32x32x16_bf16(lds_b128(av + 64 * t), __builtin_bit_cast(bf16x8, p0), o, 0, 0, 0);
    o = __builtin_amdgcn_mfma_f32_32x32x16_bf16(lds_b128(av + 64 * t + 32), __builtin_bit_cast(bf16x8, p1), o, 0, 0, 0);
  };
#pragma unroll
  for (int t = 0; t < 8; ++t) {
    unsigned pk[8];
#pragma unroll
    for (int r = 0; r < 8; ++r)
      pk[r] = pk2(__builtin_amdgcn_exp2f(sc[t][2 * r] - m), __builtin_amdgcn_exp2f(sc[t][2 * r + 1] - m));
    if (t == 0) {
      unsigned pd[8], pf[8];
#pragma unroll
      for (int r = 0; r < 8; ++r) {
        const int j = 16 * (r >> 2) + 2 * (r & 3);
        unsigned mk = (j + 1 <= dd) ? 0xffffffffu : ((j <= dd) ? 0x0000ffffu : 0u);
        pd[r] = pk[r] & mk;
        pf[r] = pk[r] ^ pd[r];
      }
      pv(0, (u32x4){pf[0], pf[1], pf[2], pf[3]}, (u32x4){pf[4], pf[5], pf[6], pf[7]});
      pv(8, (u32x4){pd[0], pd[1], pd[2], pd[3]}, (u32x4){pd[4], pd[5], pd[6], pd[7]});
    } else {
      pv(t, (u32x4){pk[0], pk[1], pk[2], pk[3]}, (u32x4){pk[4], pk[5], pk[6], pk[7]});
    }
  }
  STAMP(1, 2);
  const float ls = __shfl(o[10], i32);
  const float inv = __builtin_amdgcn_rcpf(ls);
  const int tok = 256 * tb + 32 * wid + i32;
  u32x4 om = {pk2(o[0] * inv, o[1] * inv), pk2(o[2] * inv, o[3] * inv), pk2(o[4] * inv, o[5] * inv), pk2(o[6] * inv, o[7] * inv)};
  st16_wt(ws + WS_OMAIN + ((size_t)(tok >> 4) * 16 + (head >> 1)) * 1024 + (2 * (head & 1) + h) * 256 + (tok & 15) * 16, om);
  if (h == 0) st4_wt(ws + WS_OTAIL + ((size_t)head * 2048 + tok) * 4, pk2(o[8] * inv, o[9] * inv));
  STAMP(1, 3);
}

constexpr int K3_NS = 7, K3_STAGE = 20480, K3_NST = 9;
constexpr int K3_AUX = K3_NS * K3_STAGE, K3_LDS = K3_AUX + 4 * 256;
__global__ __launch_bounds__(512, 2) void k_proj(const u16* __restrict__ x, const u16* __restrict__ wo, char* __restrict__ ws PROF_ARG) {
  extern __shared__ __attribute__((aligned(16))) char smem[];
  STAMP(2, 0);
  const int wg = blockIdx.x, tid = threadIdx.x, l = tid & 63;
  const int wid = __builtin_amdgcn_readfirstlane(tid >> 6);
  const int idx = wg >> 3;
  const int tt = (wg & 7) * 4 + (idx & 3), ct = idx >> 2;
  const unsigned sb = (unsigned)(size_t)smem;
  if (wid >= 4) {
    const int lw = wid - 4;
    unsigned vm[3], vt[3];
#pragma unroll
    for (int n = 0; n < 3; ++n) {
      const int c = 64 * (3 * lw + n) + l, R = c / 12, pos = c - 12 * R, rho = R & 31, ks = R >> 5;
      const int ch = pos ^ (((rho >> 2) & 1) << 1);
      vm[n] = (36 * ks + 18 * ((rho >> 3) & 1) + 8 * (rho >> 4) + (rho & 7)) * 1152 + ch * 16;
      const int hd = 16 * ks + 4 * ((rho >> 2) & 3) + 2 * (rho >> 4) + ((rho >> 1) & 1);
      vt[n] = (18 * hd + 16 + (rho & 1)) * 1152 + ch * 16;
    }
    const char* bw = (const char*)wo + ct * 192;
    const char* bo = ws + WS_OMAIN + (size_t)(4 * tt + lw) * 16384;
    const char* bt = ws + WS_OTAIL + tt * 256;
    auto issue = [&](int s, int sl) {
      const unsigned slot = sb + sl * K3_STAGE;
      if (s < 8) {
        glds<0>(bo + s * 2048, l * 16, slot + lw * 2048);
        glds<1024>(bo + s * 2048, l * 16, slot + lw * 2048);
#pragma unroll
        for (int n = 0; n < 3; ++n) glds<0>(bw + s * 72 * 1152, vm[n], slot + 8192 + (3 * lw + n) * 1024);
      } else {
        const unsigned v = (8 * lw + (l >> 4)) * 8192 + (l & 15) * 16;
        glds<0>(bt, v, slot + lw * 2048);
        glds<0>(bt + 4 * 8192, v, slot + lw * 2048 + 1024);
#pragma unroll
        for (int n = 0; n < 3; ++n) glds<0>(bw, vt[n], slot + 8192 + (3 * lw + n) * 1024);
      }
    };
    for (int s = 0; s < 6; ++s) issue(s, s);
    wait_vm<25>(); bar();
    issue(6, 6); wait_vm<25>(); bar();
    issue(7, 0); wait_vm<25>(); bar();
    issue(8, 1); wait_vm<25>(); bar();
    wait_vm<20>(); bar();
    wait_vm<15>(); bar();
    wait_vm<10>(); bar();
    wait_vm<5>(); bar();
    wait_vm<0>(); bar();
    return;
  }
  const int mh = wid >> 1, nh = wid & 1, i16 = l & 15, g = l >> 4, q = (l & 15) >> 2, p = l & 3;
  bf16x8 xr[2][2];
#pragma unroll
  for (int m = 0; m < 2; ++m)
#pragma unroll
    for (int w = 0; w < 2; ++w)
      xr[m][w] = *(const bf16x8*)(x + (size_t)(64 * tt + 32 * mh + 16 * m + i16) * 576 + 96 * ct + 48 * nh + 16 * w + 8 * g);
  unsigned aw[3];
#pragma unroll
  for (int c = 0; c < 3; ++c) aw[c] = 8192 + (4 * g + q) * 192 + (((3 * nh + c) ^ (g & 1)) * 32) + 8 * p;
  const unsigned ao = (2 * mh) * 2048 + l * 16;
  f32x4 acc[2][3];
  {
    u32x4 s0, s1;
#pragma unroll
    for (int e = 0; e < 4; ++e) {
      const int k = 8 * g + 2 * e;
      s0[e] = (k == i16 ? 0x3f80u : 0u) | (k + 1 == i16 ? 0x3f800000u : 0u);
      s1[e] = (k == i16 + 16 ? 0x3f80u : 0u) | (k + 1 == i16 + 16 ? 0x3f800000u : 0u);
    }
    const bf16x8 lo = __builtin_bit_cast(bf16x8, s0), hi = __builtin_bit_cast(bf16x8, s1);
    const f32x4 z = {0, 0, 0, 0};
#pragma unroll
    for (int m = 0; m < 2; ++m) {
      acc[m][0] = mfma16(xr[m][0], lo, z);
      acc[m][1] = mfma16(xr[m][0], hi, z);
      acc[m][2] = mfma16(xr[m][1], hi, z);
    }
  }
  bar();
  STAMP(2, 1);
  int sl = 0;
  for (int s = 0; s < 8; ++s) {
    const unsigned slot = sb + sl * K3_STAGE;
    bf16x8 oa[2][2], wb[2][3];
#pragma unroll
    for (int ks = 0; ks < 2; ++ks) {
#pragma unroll
      for (int m = 0; m < 2; ++m) oa[ks][m] = lds_b128(slot + ao + m * 2048 + ks * 1024);
#pragma unroll
      for (int c = 0; c < 3; ++c) wb[ks][c] = cat(lds_tr(slot + aw[c] + ks * 6144), lds_tr(slot + aw[c] + ks * 6144 + 3072));
    }
#pragma unroll
    for (int ks = 0; ks < 2; ++ks)
#pragma unroll
      for (int m = 0; m < 2; ++m)
#pragma unroll
        for (int c = 0; c < 3; ++c) acc[m][c] = mfma16(oa[ks][m], wb[ks][c], acc[m][c]);
    wait_lgkm0(); bar();
    sl = sl == K3_NS - 1 ? 0 : sl + 1;
  }
  {
    const unsigned slot = sb + 1 * K3_STAGE;
#pragma unroll
    for (int ks = 0; ks < 2; ++ks) {
      bf16x8 oa[2], wb[3];
#pragma unroll
      for (int m = 0; m < 2; ++m) {
        u32x4 t;
#pragma unroll
        for (int e = 0; e < 4; ++e) t[e] = *(const LDS3 unsigned*)(slot + (16 * ks + 4 * g + e) * 256 + (32 * mh + 16 * m + i16) * 4);
        oa[m] = __builtin_bit_cast(bf16x8, t);
      }
#pragma unroll
      for (int c = 0; c < 3; ++c) wb[c] = cat(lds_tr(slot + aw[c] + ks * 6144), lds_tr(slot + aw[c] + ks * 6144 + 3072));
#pragma unroll
      for (int m = 0; m < 2; ++m)
#pragma unroll
        for (int c = 0; c < 3; ++c) acc[m][c] = mfma16(oa[m], wb[c], acc[m][c]);
    }
  }
  STAMP(2, 2);
  u64* stats = (u64*)(ws + WS_STATS) + (size_t)(2 * tt + mh) * 12 * 32;
  {
    float mysum = 0, mym2 = 0;
#pragma unroll
    for (int m = 0; m < 2; ++m)
#pragma unroll
      for (int j = 0; j < 4; ++j) {
        const float s = row16_sum(acc[m][0][j] + acc[m][1][j] + acc[m][2][j]);
        const float mu = s * (1.f / 48.f);
        const float a = acc[m][0][j] - mu, b = acc[m][1][j] - mu, c = acc[m][2][j] - mu;
        const float m2 = row16_sum(a * a + b * b + c * c);
        if (i16 == 4 * m + j) { mysum = s; mym2 = m2; }
      }
    if (i16 < 8) {
      const int row = 16 * (i16 >> 2) + 4 * g + (i16 & 3);
      st8_wt(stats + (2 * ct + nh) * 32 + row, (u32x2){__float_as_uint(mysum), __float_as_uint(mym2) | 0x80000000u});
    }
  }
  const size_t fo = ((size_t)(2 * tt + mh) * 36 + 6 * ct + 3 * nh) * 1024 + l * 16;
#pragma unroll
  for (int c = 0; c < 3; ++c) {
    u32x4 hv = {pk2(acc[0][c][0], acc[0][c][1]), pk2(acc[0][c][2], acc[0][c][3]), pk2(acc[1][c][0], acc[1][c][1]), pk2(acc[1][c][2], acc[1][c][3])};
    st16_wt(ws + WS_HT + fo + c * 1024, hv);
  }
  float ps[6], pm[6];
  {
    const u64* src = stats + (6 * (l >> 5)) * 32 + (l & 31);
    for (int spin = 0; spin < (1 << 20); ++spin) {
      bool ok = true;
#pragma unroll
      for (int k = 0; k < 6; ++k) {
        const u64 v = __hip_atomic_load(src + k * 32, __ATOMIC_RELAXED, __HIP_MEMORY_SCOPE_AGENT);
        ps[k] = __uint_as_float((unsigned)v);
        pm[k] = __uint_as_float((unsigned)(v >> 32) & 0x7fffffffu);
        ok &= (v >> 63) != 0;
      }
      if (__all(ok)) break;
      __builtin_amdgcn_s_sleep(1);
    }
  }
  STAMP(2, 3);
  {
    float s = ((ps[0] + ps[1]) + (ps[2] + ps[3])) + (ps[4] + ps[5]);
    s += __shfl_xor(s, 32);
    const float mu = s * (1.f / 576.f);
    float m2 = 0;
#pragma unroll
    for (int k = 0; k < 6; ++k) { const float d = ps[k] * (1.f / 48.f) - mu; m2 += pm[k] + 48.f * d * d; }
    m2 += __shfl_xor(m2, 32);
    const float var = m2 * (1.f / 576.f);
    const float r1 = __builtin_amdgcn_rsqf(var + 1e-5f);
    const float r2 = __builtin_amdgcn_rsqf(var * r1 * r1 + 1e-5f);
    const unsigned a = sb + K3_AUX + wid * 256;
    if (l < 32) *(LDS3 f32x2*)(a + l * 8) = (f32x2){mu, r1 * r2};
    wait_lgkm0();
    f32x4 st[2][2];
#pragma unroll
    for (int m = 0; m < 2; ++m)
#pragma unroll
      for (int k = 0; k < 2; ++k) st[m][k] = *(const LDS3 f32x4*)(a + (16 * m + 4 * g + 2 * k) * 8);
#pragma unroll
    for (int c = 0; c < 3; ++c) {
      float v[8];
#pragma unroll
      for (int e = 0; e < 8; ++e) v[e] = (acc[e >> 2][c][e & 3] - st[e >> 2][(e >> 1) & 1][(e & 1) * 2]) * st[e >> 2][(e >> 1) & 1][(e & 1) * 2 + 1];
      u32x4 hv = {pk2(v[0], v[1]), pk2(v[2], v[3]), pk2(v[4], v[5]), pk2(v[6], v[7])};
      st16_wt(ws + WS_HNT + fo + c * 1024, hv);
    }
  }
  STAMP(2, 4);
}

constexpr int G1_NS = 5, G1_STAGE = 28672, G1_NST = 32, G1_LDS = G1_NS * G1_STAGE;
DEV float gelu_t(float u) {
  const float k1 = -2.f * 0.7978845608028654f * 1.4426950408889634f, k2 = k1 * 0.044715f;
  const float e = __builtin_amdgcn_exp2f(u * (k1 + k2 * u * u));
  return u * __builtin_amdgcn_rcpf(1.f + e);
}
__global__ __launch_bounds__(512, 2) void k_mlp1(const u16* __restrict__ w1, const u16* __restrict__ b1, char* __restrict__ ws PROF_ARG) {
  extern __shared__ __attribute__((aligned(16))) char smem[];
  STAMP(3, 0);
  const int wg = blockIdx.x, tid = threadIdx.x, l = tid & 63;
  const int wid = __builtin_amdgcn_readfirstlane(tid >> 6);
  const int tile = (wg & 7) * 27 + (wg >> 3), ft = tile / 6, dt = tile - 6 * ft;
  const unsigned sb = (unsigned)(size_t)smem;
  if (wid >= 4) {
    const int lw = wid - 4;
    const int f = 4 * (lw & 1) + (l >> 4);
    const unsigned vb = (4 * lw + (l >> 4)) * 9216 + (((l & 15) ^ (f << 1)) * 16);
    const char* pb = (const char*)w1 + ft * 256;
    const char* pa = ws + WS_HNT + (size_t)((lw >> 1) * 36 + 6 * dt + 3 * (lw & 1)) * 1024;
    unsigned slot = sb;
    auto issue = [&]() {
      glds<0>(pa, l * 16, slot + lw * 3072);
      glds<1024>(pa, l * 16, slot + lw * 3072);
      glds<2048>(pa, l * 16, slot + lw * 3072);
#pragma unroll
      for (int i = 0; i < 4; ++i) glds_nt<0>(pb + i * 16 * 9216, vb, slot + 12288 + (lw + 4 * i) * 1024);
      pa += 72 * 1024; pb += 64 * 9216;
      slot = slot == sb + (G1_NS - 1) * G1_STAGE ? sb : slot + G1_STAGE;
    };
    for (int s = 0; s < G1_NS - 1; ++s) issue();
    wait_vm<21>(); bar();
    for (int s = 0; s < G1_NST - (G1_NS - 1); ++s) { issue(); wait_vm<21>(); bar(); }
    wait_vm<14>(); bar();
    wait_vm<7>(); bar();
    wait_vm<0>(); bar();
    return;
  }
  const int mh = wid >> 1, nh = wid & 1, g = l >> 4, q = (l & 15) >> 2, p = l & 3;
  u32x2 bias[4];
#pragma unroll
  for (int c = 0; c < 4; ++c) bias[c] = *(const u32x2*)(b1 + 128 * ft + 64 * nh + 16 * c + 4 * g);
  const unsigned aa = 3 * mh * 1024 + l * 16;
  unsigned aw[4];
  const int f = 4 * (g & 1) + q;
#pragma unroll
  for (int c = 0; c < 4; ++c) aw[c] = 12288 + (4 * g + q) * 256 + (((4 * nh + c) ^ f) * 32) + 8 * p;
  f32x4 acc[4][3];
#pragma unroll
  for (int c = 0; c < 4; ++c)
#pragma unroll
    for (int u = 0; u < 3; ++u) acc[c][u] = (f32x4){0, 0, 0, 0};
  bf16x8 ha[2][3], wb[2][4];
  auto rd = [&](int ks, unsigned slot) {
#pragma unroll
    for (int u = 0; u < 3; ++u) ha[ks][u] = lds_b128(slot + aa + ks * 6144 + u * 1024);
#pragma unroll
    for (int c = 0; c < 4; ++c) wb[ks][c] = cat(lds_tr(slot + aw[c] + ks * 8192), lds_tr(slot + aw[c] + ks * 8192 + 4096));
  };
  auto mm = [&](int ks) {
#pragma unroll
    for (int c = 0; c < 4; ++c)
#pragma unroll
      for (int u = 0; u < 3; ++u) acc[c][u] = mfma16(wb[ks][c], ha[ks][u], acc[c][u]);
  };
  auto ilv = [&]() {
#pragma unroll
    for (int k = 0; k < 11; ++k) { __builtin_amdgcn_sched_group_barrier(0x8, 1, 0); __builtin_amdgcn_sched_group_barrier(0x100, 1, 0); }
    __builtin_amdgcn_sched_group_barrier(0x8, 1, 0);
  };
  bar();
  STAMP(3, 1);
  rd(0, sb); rd(1, sb); mm(0);
  unsigned slot = sb + G1_STAGE;
  for (int s = 1; s < G1_NST; ++s) {
    wait_lgkm0(); bar();
    __builtin_amdgcn_sched_barrier(0);
    rd(0, slot); mm(1); ilv();
    __builtin_amdgcn_sched_barrier(0);
    rd(1, slot); mm(0); ilv();
    __builtin_amdgcn_sched_barrier(0);
    slot = slot == sb + (G1_NS - 1) * G1_STAGE ? sb : slot + G1_STAGE;
  }
  mm(1);
  STAMP(3, 2);
#pragma unroll
  for (int a = 0; a < 2; ++a)
#pragma unroll
    for (int u = 0; u < 3; ++u) {
      float v[8];
#pragma unroll
      for (int e = 0; e < 8; ++e) {
        const unsigned bw = bias[2 * a + (e >> 2)][(e >> 1) & 1];
        v[e] = gelu_t(acc[2 * a + (e >> 2)][u][e & 3] + ((e & 1) ? bfhi(bw) : bflo(bw)));
      }
      u32x4 o = {pk2(v[0], v[1]), pk2(v[2], v[3]), pk2(v[4], v[5]), pk2(v[6], v[7])};
      st16_wt(ws + WS_UT + ((size_t)(4 * ft + 2 * nh + a) * 36 + 6 * dt + 3 * mh + u) * 1024 + l * 16, o);
    }
  STAMP(3, 3);
}

constexpr int G2_NS = 4, G2_STAGE = 34816, G2_NST = 18, G2_LDS = G2_NS * G2_STAGE;
template <int NA>
DEV void g2_loader(const char* pa, const char* pb, unsigned vb, unsigned sb, unsigned aoff, int lw, int l) {
  int sl = 0;
  auto issue = [&]() {
    const unsigned slot = sb + sl * G2_STAGE;
    glds<0>(pa, l * 16, slot + aoff);
    glds<1024>(pa, l * 16, slot + aoff);
    glds<2048>(pa, l * 16, slot + aoff);
    glds<3072>(pa, l * 16, slot + aoff);
    if (NA == 5) glds<0>(pa + 4096, l * 16, slot + aoff + 4096);
#pragma unroll
    for (int i = 0; i < 4; ++i) glds_nt<0>(pb + i * 16 * 4096, vb, slot + 18432 + (lw + 4 * i) * 1024);
    pa += 72 * 1024; pb += 64 * 4096;
    sl = (sl + 1) & (G2_NS - 1);
  };
  for (int s = 0; s < G2_NS - 1; ++s) issue();
  wait_vm<2 * (NA + 4)>(); bar();
  for (int s = 0; s < G2_NST - (G2_NS - 1); ++s) { issue(); wait_vm<2 * (NA + 4)>(); bar(); }
  wait_vm<NA + 4>(); bar();
  wait_vm<0>(); bar();
}
__global__ __launch_bounds__(512, 2) void k_mlp2(const u16* __restrict__ w2, const u16* __restrict__ b2, char* __restrict__ ws, u16* __restrict__ out PROF_ARG) {
  extern __shared__ __attribute__((aligned(16))) char smem[];
  STAMP(4, 0);
  const int wg = blockIdx.x, tid = threadIdx.x, l = tid & 63;
  const int wid = __builtin_amdgcn_readfirstlane(tid >> 6);
  const int xcd = wg & 7, idx = wg >> 3;
  const int kq = xcd >> 1, st = (xcd & 1) * 8 + (idx >> 2), dt = idx & 3;
  const int tile = st * 4 + dt;
  const unsigned sb = (unsigned)(size_t)smem;
  const int g = l >> 4, q = (l & 15) >> 2, p = l & 3;
  char* part = ws + WS_PART + (size_t)tile * 16 * 9216;
  if (wid >= 4) {
    const int lw = wid - 4;
    const int f = 4 * (lw & 1) + (l >> 4);
    const unsigned vb = (4 * lw + (l >> 4)) * 4096 + (((l & 15) ^ (f << 1)) * 16);
    const char* pb = (const char*)w2 + (size_t)kq * 1152 * 4096 + st * 256;
    const int ks = lw & 1, u0 = (lw >> 1) * 5;
    const char* pa = ws + WS_UT + (size_t)((36 * kq + ks) * 36 + 9 * dt + u0) * 1024;
    const unsigned aoff = (9 * ks + u0) * 1024;
    if (lw < 2) g2_loader<5>(pa, pb, vb, sb, aoff, lw, l); else g2_loader<4>(pa, pb, vb, sb, aoff, lw, l);
  } else {
    const unsigned aa = l * 16;
    unsigned aw[2];
    const int f = 4 * (g & 1) + q;
#pragma unroll
    for (int c = 0; c < 2; ++c) aw[c] = 18432 + (4 * g + q) * 256 + (((2 * wid + c) ^ f) * 32) + 8 * p;
    f32x4 acc[2][9];
#pragma unroll
    for (int c = 0; c < 2; ++c)
#pragma unroll
      for (int u = 0; u < 9; ++u) acc[c][u] = (f32x4){0, 0, 0, 0};
    bf16x8 ua[2][9], wb[2][2];
    auto rd = [&](int ks, unsigned slot) {
#pragma unroll
      for (int c = 0; c < 2; ++c) wb[ks][c] = cat(lds_tr(slot + aw[c] + ks * 8192), lds_tr(slot + aw[c] + ks * 8192 + 4096));
#pragma unroll
      for (int u = 0; u < 9; ++u) ua[ks][u] = lds_b128(slot + aa + ks * 9216 + u * 1024);
    };
    auto mm = [&](int ks) {
#pragma unroll
      for (int u = 0; u < 9; ++u)
#pragma unroll
        for (int c = 0; c < 2; ++c) acc[c][u] = mfma16(wb[ks][c], ua[ks][u], acc[c][u]);
    };
    auto ilv = [&]() {
#pragma unroll
      for (int k = 0; k < 13; ++k) { __builtin_amdgcn_sched_group_barrier(0x8, 1, 0); __builtin_amdgcn_sched_group_barrier(0x100, 1, 0); }
      __builtin_amdgcn_sched_group_barrier(0x8, 5, 0);
    };
    bar();
    STAMP(4, 1);
    rd(0, sb); rd(1, sb); mm(0);
    for (int s = 1; s < G2_NST; ++s) {
      const unsigned slot = sb + (s & (G2_NS - 1)) * G2_STAGE;
      wait_lgkm0(); bar();
      __builtin_amdgcn_sched_barrier(0);
      rd(0, slot); mm(1); ilv();
      __builtin_amdgcn_sched_barrier(0);
      rd(1, slot); mm(0); ilv();
      __builtin_amdgcn_sched_barrier(0);
    }
    mm(1);
    STAMP(4, 2);
    if (wid == kq) {
#pragma unroll
      for (int u = 0; u < 9; ++u) {
        *(LDS3 f32x4*)(sb + 2 * G2_STAGE + u * 2048 + l * 16) = acc[0][u];
        *(LDS3 f32x4*)(sb + 2 * G2_STAGE + u * 2048 + 1024 + l * 16) = acc[1][u];
      }
    } else {
#pragma unroll
      for (int u = 0; u < 9; ++u) {
        u32x4 o = {pk2(acc[0][u][0], acc[0][u][1]), pk2(acc[0][u][2], acc[0][u][3]), pk2(acc[1][u][0], acc[1][u][1]), pk2(acc[1][u][2], acc[1][u][3])};
        st16_wt(part + (wid * 4 + kq) * 9216 + u * 1024 + l * 16, o);
      }
      wait_vm<0>();
      if (l == 0) __hip_atomic_store((unsigned*)(ws + WS_FLAGS + tile * 1024 + (wid * 4 + kq) * 64), 1u, __ATOMIC_RELAXED, __HIP_MEMORY_SCOPE_AGENT);
    }
  }
  const int sstep = 4 * st + kq;
  const u32x2 bb0 = *(const u32x2*)(b2 + 32 * sstep + 4 * g), bb1 = *(const u32x2*)(b2 + 32 * sstep + 16 + 4 * g);
  const char* hT = ws + WS_HT + ((size_t)sstep * 36 + 9 * dt) * 1024 + l * 16;
  u32x4 hv0 = *(const u32x4*)(hT + wid * 1024), hv1 = *(const u32x4*)(hT + 8 * 1024);
  if (wid == 7) {
    const unsigned* fl = (const unsigned*)(ws + WS_FLAGS + tile * 1024 + (kq * 4 + (l & 3)) * 64);
    for (int spin = 0; spin < (1 << 20); ++spin) {
      const unsigned v = __hip_atomic_load(fl, __ATOMIC_RELAXED, __HIP_MEMORY_SCOPE_AGENT);
      if (__all(v == 1u || (l & 3) == kq)) break;
      __builtin_amdgcn_s_sleep(1);
    }
  }
  wait_lgkm0(); bar();
  STAMP(4, 3);
  const unsigned tb_ = sb + 3 * G2_STAGE;
  auto fin = [&](int u, u32x4 hv) {
    u32x4 pv[3];
#pragma unroll
    for (int k = 0; k < 3; ++k) {
      const int src = k + (k >= kq ? 1 : 0);
      const char* a = part + (kq * 4 + src) * 9216 + u * 1024 + l * 16;
      asm volatile("global_load_dwordx4 %0, %1, off sc1" : "=&v"(pv[k]) : "v"(a) : "memory");
    }
    const f32x4 a0 = *(const LDS3 f32x4*)(sb + 2 * G2_STAGE + u * 2048 + l * 16);
    const f32x4 a1 = *(const LDS3 f32x4*)(sb + 2 * G2_STAGE + u * 2048 + 1024 + l * 16);
    asm volatile("s_waitcnt vmcnt(0)" : "+v"(pv[0]), "+v"(pv[1]), "+v"(pv[2])::"memory");
    float v[8];
#pragma unroll
    for (int e = 0; e < 8; ++e) {
      const int w = e >> 1;
      const unsigned bw = e < 4 ? bb0[w] : bb1[w - 2];
      float t = e < 4 ? a0[e] : a1[e - 4];
      if (e & 1) t += bfhi(pv[0][w]) + bfhi(pv[1][w]) + bfhi(pv[2][w]) + bfhi(bw) + bfhi(hv[w]);
      else t += bflo(pv[0][w]) + bflo(pv[1][w]) + bflo(pv[2][w]) + bflo(bw) + bflo(hv[w]);
      v[e] = t;
    }
    const unsigned a = tb_ + (16 * u + (l & 15)) * 72 + 8 * g;
    *(LDS3 u32x2*)(a) = (u32x2){pk2(v[0], v[1]), pk2(v[2], v[3])};
    *(LDS3 u32x2*)(a + 32) = (u32x2){pk2(v[4], v[5]), pk2(v[6], v[7])};
    wait_lgkm0();
    const unsigned r = tb_ + (16 * u + 8 * (g >> 1) + q) * 72 + 32 * (g & 1) + 8 * p;
    const bf16x8 o = cat(lds_tr(r), lds_tr(r + 4 * 72));
    *(bf16x8*)(out + (size_t)(32 * sstep + 16 * (g & 1) + (l & 15)) * 576 + 144 * dt + 16 * u + 8 * (g >> 1)) = o;
  };
  fin(wid, hv0);
  if (wid == 4) fin(8, hv1);
  STAMP(4, 4);
}

extern "C" void kernel_launch(void* const* d_in, const int* in_sizes, int n_in, void* d_out, int out_size, void* d_ws, size_t ws_size,
                              hipStream_t stream) {
  const u16* x = (const u16*)d_in[0];
  const u16* wqkv = (const u16*)d_in[1];
  const u16* wo = (const u16*)d_in[2];
  const u16* w1 = (const u16*)d_in[4];
  const u16* b1 = (const u16*)d_in[5];
  const u16* w2 = (const u16*)d_in[6];
  const u16* b2 = (const u16*)d_in[7];
  char* ws = (char*)d_ws;
  (void)hipFuncSetAttribute((const void*)k_qkv, hipFuncAttributeMaxDynamicSharedMemorySize, K1_LDS);
  (void)hipFuncSetAttribute((const void*)k_proj, hipFuncAttributeMaxDynamicSharedMemorySize, K3_LDS);
  (void)hipFuncSetAttribute((const void*)k_mlp1, hipFuncAttributeMaxDynamicSharedMemorySize, G1_LDS);
  (void)hipFuncSetAttribute((const void*)k_mlp2, hipFuncAttributeMaxDynamicSharedMemorySize, G2_LDS);
  k_qkv<<<256, 512, K1_LDS, stream>>>(x, wqkv, ws PROF_PASS);
  k_attn<<<256, 512, A_LDS, stream>>>(ws PROF_PASS);
  k_proj<<<192, 512, K3_LDS, stream>>>(x, wo, ws PROF_PASS);
  k_mlp1<<<216, 512, G1_LDS, stream>>>(w1, b1, ws PROF_PASS);
  k_mlp2<<<256, 512, G2_LDS, stream>>>(w2, b2, ws, (u16*)d_out PROF_PASS);
}
```

```cpp
#include <hip/hip_runtime.h>
#include <hip/hip_bf16.h>
#include <stdint.h>
#include <type_traits>

typedef unsigned short u16;
typedef unsigned long long u64;
typedef __attribute__((ext_vector_type(8))) __bf16 bf16x8;
typedef __attribute__((ext_vector_type(4))) __bf16 bf16x4;
typedef __attribute__((ext_vector_type(2))) __bf16 bf16x2;
typedef __attribute__((ext_vector_type(4))) short s16x4;
typedef __attribute__((ext_vector_type(4))) float f32x4;
typedef __attribute__((ext_vector_type(2))) float f32x2;
typedef __attribute__((ext_vector_type(8))) float f32x8;
typedef __attribute__((ext_vector_type(16))) float f32x16;
typedef __attribute__((ext_vector_type(4))) unsigned u32x4;
typedef __attribute__((ext_vector_type(2))) unsigned u32x2;
#define LDS3 __attribute__((address_space(3)))
#define DEV __device__ __forceinline__

constexpr size_t WS_ROWST = 524288;
constexpr size_t WS_COLSM = WS_ROWST + 2048 * 16;
constexpr size_t WS_STATS = WS_COLSM + 16384;
#define QRS 5248
constexpr int QKV_RS = QRS;
constexpr size_t WS_QKVT = 1048576;
constexpr size_t WS_OMAIN = WS_QKVT + (size_t)1728 * QKV_RS;
constexpr size_t WS_OTAIL = WS_OMAIN + (size_t)128 * 16 * 1024;
constexpr size_t WS_HT = WS_OTAIL + (size_t)32 * 2048 * 4;
constexpr size_t WS_HNT = WS_HT + (size_t)64 * 36 * 1024;
constexpr size_t WS_UT = WS_HNT + (size_t)64 * 36 * 1024;
constexpr size_t WS_PART = WS_UT + (size_t)144 * 36 * 1024;
constexpr size_t WS_END = WS_PART + (size_t)64 * 16 * 9 * 1024;
constexpr int FPI = 256, SPI = 512;
constexpr size_t WS_F_QKV = FPI, WS_F_ATT = 256 * FPI, WS_F_PRJ = 512 * FPI, WS_F_ML1 = 768 * FPI, WS_F_RED = 1024 * FPI;
static_assert(WS_F_RED + 1024 * FPI <= WS_ROWST && WS_STATS + 64 * 12 * SPI <= WS_QKVT, "ws map");

#define W1F 0
#define W2F 0
#define PF0 0
#define PFWO 1
#define PFA 15
#define PFB 1
#define NOWO 192
#define PFT 2
#define PB1 2
#define B2E 0
#define OUTF 0
#define PP 31
#define PS 3
#define PSL 1
#define SS0 8
#define SS1 0
#define HL 0
#define DL1 0
#define DL2 0
#define DL4 0
#define DL8 0
#define EB 5
#define EB2 8
#define PFH 2
#define W2E 144
#define PQ 0
#define ROT 1
#define PFN 0
#define PFD 0
#define BS1 8
#define VSPLIT 1
#define ATN 2
#define W1E 80
#define PFI 2
#define PFC 0

template <int OFF, int F = 0>
DEV void glds(const void* sbase, unsigned voff, unsigned lds) {
  if (F == 0) asm volatile("s_mov_b32 m0, %2\n\ts_nop 0\n\tglobal_load_lds_dwordx4 %0, %1 offset:%3" ::"v"(voff), "s"(sbase), "s"(lds), "i"(OFF) : "memory");
  if (F == 1) asm volatile("s_mov_b32 m0, %2\n\ts_nop 0\n\tglobal_load_lds_dwordx4 %0, %1 offset:%3 nt" ::"v"(voff), "s"(sbase), "s"(lds), "i"(OFF) : "memory");
  if (F == 2) asm volatile("s_mov_b32 m0, %2\n\ts_nop 0\n\tglobal_load_lds_dwordx4 %0, %1 offset:%3 sc1" ::"v"(voff), "s"(sbase), "s"(lds), "i"(OFF) : "memory");
}
DEV void glds_m(const void* sbase, unsigned voff, unsigned lds, u64 lanes) {
  asm volatile("s_mov_b32 m0, %2\n\ts_mov_b64 exec, %3\n\tglobal_load_lds_dwordx4 %0, %1\n\ts_mov_b64 exec, -1" ::"v"(voff), "s"(sbase), "s"(lds), "s"(lanes) : "memory");
}
template <int F = 0>
DEV void touch(const void* vaddr, unsigned lds) {
  if (F == 0) asm volatile("s_mov_b32 m0, %1\n\ts_nop 0\n\tglobal_load_lds_dword %0, off" ::"v"(vaddr), "s"(lds) : "memory");
  if (F == 1) asm volatile("s_mov_b32 m0, %1\n\ts_nop 0\n\tglobal_load_lds_dword %0, off nt" ::"v"(vaddr), "s"(lds) : "memory");
}
template <int N> DEV void wait_vm() { asm volatile("s_waitcnt vmcnt(%0)" ::"i"(N) : "memory"); }
DEV void wait_lgkm0() { asm volatile("s_waitcnt lgkmcnt(0)" ::: "memory"); }
DEV void bar() { asm volatile("" ::: "memory"); __builtin_amdgcn_s_barrier(); asm volatile("" ::: "memory"); }
DEV bf16x8 lds_b128(unsigned a) { return *(const LDS3 bf16x8*)a; }
DEV bf16x4 lds_tr(unsigned a) { return __builtin_amdgcn_ds_read_tr16_b64_v4bf16((LDS3 bf16x4*)a); }
DEV bf16x8 cat(bf16x4 a, bf16x4 b) { return __builtin_shufflevector(a, b, 0, 1, 2, 3, 4, 5, 6, 7); }
DEV f32x4 mfma16(bf16x8 a, bf16x8 b, f32x4 c) { return __builtin_amdgcn_mfma_f32_16x16x32_bf16(a, b, c, 0, 0, 0); }
DEV unsigned pk2(float a, float b) { bf16x2 p = {(__bf16)a, (__bf16)b}; return __builtin_bit_cast(unsigned, p); }
DEV float bflo(unsigned u) { return __uint_as_float(u << 16); }
DEV float bfhi(unsigned u) { return __uint_as_float(u & 0xffff0000u); }
DEV void st16_wt(void* p, u32x4 v) { asm volatile("global_store_dwordx4 %0, %1, off sc1\n\ts_nop 1" ::"v"(p), "v"(v) : "memory"); }
template <int OFF> DEV void st16_s(const void* sbase, unsigned voff, u32x4 v) {
  asm volatile("global_store_dwordx4 %0, %1, %2 offset:%3 sc1\n\ts_nop 1" ::"v"(voff), "v"(v), "s"(sbase), "i"(OFF) : "memory");
}
DEV void st_out(void* p, bf16x8 v) {
  if (OUTF == 0) asm volatile("global_store_dwordx4 %0, %1, off\n\ts_nop 1" ::"v"(p), "v"(v) : "memory");
  if (OUTF == 1) asm volatile("global_store_dwordx4 %0, %1, off sc1\n\ts_nop 1" ::"v"(p), "v"(v) : "memory");
  if (OUTF == 2) asm volatile("global_store_dwordx4 %0, %1, off sc0 sc1\n\ts_nop 1" ::"v"(p), "v"(v) : "memory");
  if (OUTF == 3) asm volatile("global_store_dwordx4 %0, %1, off nt\n\ts_nop 1" ::"v"(p), "v"(v) : "memory");
}
DEV void st8_wt(void* p, u32x2 v) { asm volatile("global_store_dwordx2 %0, %1, off sc1\n\ts_nop 1" ::"v"(p), "v"(v) : "memory"); }
DEV void st4_wt(void* p, unsigned v) { asm volatile("global_store_dword %0, %1, off sc1\n\ts_nop 1" ::"v"(p), "v"(v) : "memory"); }
DEV u32x4 ld16_dev(const void* p) { u32x4 v; asm volatile("global_load_dwordx4 %0, %1, off sc1\n\ts_waitcnt vmcnt(0)" : "=&v"(v) : "v"(p) : "memory"); return v; }
DEV u32x2 ld8_dev(const void* p) { u32x2 v; asm volatile("global_load_dwordx2 %0, %1, off sc1\n\ts_waitcnt vmcnt(0)" : "=&v"(v) : "v"(p) : "memory"); return v; }
template <int CTRL> DEV float dpp_add(float v) {
  return v + __builtin_bit_cast(float, __builtin_amdgcn_update_dpp(0, __builtin_bit_cast(int, v), CTRL, 0xf, 0xf, true));
}
DEV float row16_sum(float v) { v = dpp_add<0x128>(v); v = dpp_add<0x124>(v); v = dpp_add<0x122>(v); v = dpp_add<0x121>(v); return v; }
DEV float dot2(unsigned a, unsigned b, float c) {
  return __builtin_amdgcn_fdot2_f32_bf16(__builtin_bit_cast(bf16x2, a), __builtin_bit_cast(bf16x2, b), c, false);
}
constexpr int SPIN_MAX = 1 << 18;
DEV void poll(const char* flag, u32x2 tag) {
#pragma unroll 1
  for (int spin = 0; spin < SPIN_MAX; ++spin) {
    const u32x2 v = ld8_dev(flag);
    if (v[0] == tag[0] && v[1] == tag[1]) break;
    __builtin_amdgcn_s_sleep(PSL);
  }
}
#define PSTEP(r) "s_waitcnt vmcnt(2)\n\tv_cmp_ne_u64 vcc, " r ", %[tag]\n\ts_and_b64 exec, exec, vcc\n\ts_cbranch_scc0 2f\n\tglobal_load_dwordx2 " r ", %[p], off sc1\n\t"
template <int ON, int D = 0> DEV void pollp(const char* flag, u32x2 tag) {
  if (D) __builtin_amdgcn_s_sleep(D);
  if (!ON) { poll(flag, tag); return; }
  unsigned cnt = SPIN_MAX; u64 ex;
  asm volatile("s_mov_b64 %[ex], exec\n\tglobal_load_dwordx2 v[250:251], %[p], off sc1\n\ts_sleep %[sl]\n\t"
               "global_load_dwordx2 v[252:253], %[p], off sc1\n\ts_sleep %[sl]\n\tglobal_load_dwordx2 v[254:255], %[p], off sc1\n"
               "1:\n\t" PSTEP("v[250:251]") PSTEP("v[252:253]") PSTEP("v[254:255]")
               "s_sub_u32 %[cnt], %[cnt], 1\n\ts_cbranch_scc0 1b\n"
               "2:\n\ts_mov_b64 exec, %[ex]"
               : [cnt] "+s"(cnt), [ex] "=&s"(ex) : [p] "v"(flag), [tag] "v"(tag), [sl] "i"(PS)
               : "memory", "scc", "vcc", "v250", "v251", "v252", "v253", "v254", "v255");
}
DEV unsigned gelu2(f32x2 u) {
  const float k1 = -2.f * 0.7978845608028654f * 1.4426950408889634f, k2 = k1 * 0.044715f;
  const f32x2 a = u * __builtin_elementwise_fma(u * u, (f32x2){k2, k2}, (f32x2){k1, k1});
  const f32x2 d = (f32x2){__builtin_amdgcn_exp2f(a[0]), __builtin_amdgcn_exp2f(a[1])} + 1.f;
  const f32x2 o = u * (f32x2){__builtin_amdgcn_rcpf(d[0]), __builtin_amdgcn_rcpf(d[1])};
  return pk2(o[0], o[1]);
}
DEV float gelu_t(float u) {
  const float k1 = -2.f * 0.7978845608028654f * 1.4426950408889634f, k2 = k1 * 0.044715f;
  const float e = __builtin_amdgcn_exp2f(u * (k1 + k2 * u * u));
  return u * __builtin_amdgcn_rcpf(1.f + e);
}
constexpr unsigned wrap_mask(int n, int ns) { unsigned m = 0; for (int s = 0; s < n; ++s) if (s % ns == ns - 1) m |= 1u << (n - 1 - s); return m; }

#define GQ_ASM \
  "v_mov_b32 v160, 0\n"\
  "v_mov_b32 v161, 0\n"\
  "v_mov_b32 v162, 0\n"\
  "v_mov_b32 v163, 0\n"\
  "v_mov_b32 v164, 0\n"\
  "v_mov_b32 v165, 0\n"\
  "v_mov_b32 v166, 0\n"\
  "v_mov_b32 v167, 0\n"\
  "v_mov_b32 v168, 0\n"\
  "v_mov_b32 v169, 0\n"\
  "v_mov_b32 v170, 0\n"\
  "v_mov_b32 v171, 0\n"\
  "v_mov_b32 v172, 0\n"\
  "v_mov_b32 v173, 0\n"\
  "v_mov_b32 v174, 0\n"\
  "v_mov_b32 v175, 0\n"\
  "v_mov_b32 v176, 0\n"\
  "v_mov_b32 v177, 0\n"\
  "v_mov_b32 v178, 0\n"\
  "v_mov_b32 v179, 0\n"\
  "v_mov_b32 v180, 0\n"\
  "v_mov_b32 v181, 0\n"\
  "v_mov_b32 v182, 0\n"\
  "v_mov_b32 v183, 0\n"\
  "v_mov_b32 v184, 0\n"\
  "v_mov_b32 v185, 0\n"\
  "v_mov_b32 v186, 0\n"\
  "v_mov_b32 v187, 0\n"\
  "v_mov_b32 v188, 0\n"\
  "v_mov_b32 v189, 0\n"\
  "v_mov_b32 v190, 0\n"\
  "v_mov_b32 v191, 0\n"\
  "1:\n"\
  "s_barrier\n"\
  "v_mfma_f32_16x16x32_bf16 v[64:67], v[160:163], v[176:179], v[64:67]\n"\
  "ds_read_b64_tr_b16 v[144:145], %[t0] offset:0\n"\
  "s_bitcmp1_b32 %[ma], %[cnt]\n"\
  "v_mfma_f32_16x16x32_bf16 v[68:71], v[164:167], v[176:179], v[68:71]\n"\
  "ds_read_b64_tr_b16 v[146:147], %[t0] offset:1024\n"\
  "s_cselect_b32 %[ta], %[wa], %[sa]\n"\
  "v_mfma_f32_16x16x32_bf16 v[72:75], v[168:171], v[176:179], v[72:75]\n"\
  "ds_read_b128 v[128:131], %[ap] offset:0\n"\
  "s_sub_u32 %[cnt], %[cnt], 1\n"\
  "v_mfma_f32_16x16x32_bf16 v[76:79], v[172:175], v[176:179], v[76:79]\n"\
  "ds_read_b128 v[132:135], %[ap] offset:512\n"\
  "v_mfma_f32_16x16x32_bf16 v[80:83], v[160:163], v[180:183], v[80:83]\n"\
  "ds_read_b128 v[136:139], %[ap] offset:4096\n"\
  "v_mfma_f32_16x16x32_bf16 v[84:87], v[164:167], v[180:183], v[84:87]\n"\
  "ds_read_b128 v[140:143], %[ap] offset:4608\n"\
  "v_mfma_f32_16x16x32_bf16 v[88:91], v[168:171], v[180:183], v[88:91]\n"\
  "ds_read_b64_tr_b16 v[148:149], %[t1] offset:0\n"\
  "v_add_u32 %[ap], %[ta], %[ap]\n"\
  "v_mfma_f32_16x16x32_bf16 v[92:95], v[172:175], v[180:183], v[92:95]\n"\
  "ds_read_b64_tr_b16 v[150:151], %[t1] offset:1024\n"\
  "v_mfma_f32_16x16x32_bf16 v[96:99], v[160:163], v[184:187], v[96:99]\n"\
  "ds_read_b64_tr_b16 v[152:153], %[t2] offset:0\n"\
  "v_mfma_f32_16x16x32_bf16 v[100:103], v[164:167], v[184:187], v[100:103]\n"\
  "ds_read_b64_tr_b16 v[154:155], %[t2] offset:1024\n"\
  "v_mfma_f32_16x16x32_bf16 v[104:107], v[168:171], v[184:187], v[104:107]\n"\
  "ds_read_b64_tr_b16 v[156:157], %[t3] offset:0\n"\
  "v_mfma_f32_16x16x32_bf16 v[108:111], v[172:175], v[184:187], v[108:111]\n"\
  "ds_read_b64_tr_b16 v[158:159], %[t3] offset:1024\n"\
  "v_mfma_f32_16x16x32_bf16 v[112:115], v[160:163], v[188:191], v[112:115]\n"\
  "v_mfma_f32_16x16x32_bf16 v[116:119], v[164:167], v[188:191], v[116:119]\n"\
  "v_mfma_f32_16x16x32_bf16 v[120:123], v[168:171], v[188:191], v[120:123]\n"\
  "v_mfma_f32_16x16x32_bf16 v[124:127], v[172:175], v[188:191], v[124:127]\n"\
  "s_waitcnt lgkmcnt(2)\n"\
  "v_mfma_f32_16x16x32_bf16 v[64:67], v[128:131], v[144:147], v[64:67]\n"\
  "ds_read_b64_tr_b16 v[176:177], %[t0] offset:8192\n"\
  "v_mfma_f32_16x16x32_bf16 v[68:71], v[132:135], v[144:147], v[68:71]\n"\
  "ds_read_b64_tr_b16 v[178:179], %[t0] offset:9216\n"\
  "v_add_u32 %[t0], %[ta], %[t0]\n"\
  "v_mfma_f32_16x16x32_bf16 v[72:75], v[136:139], v[144:147], v[72:75]\n"\
  "ds_read_b128 v[160:163], %[aq] offset:0\n"\
  "v_mfma_f32_16x16x32_bf16 v[76:79], v[140:143], v[144:147], v[76:79]\n"\
  "ds_read_b128 v[164:167], %[aq] offset:512\n"\
  "v_mfma_f32_16x16x32_bf16 v[80:83], v[128:131], v[148:151], v[80:83]\n"\
  "ds_read_b128 v[168:171], %[aq] offset:4096\n"\
  "v_mfma_f32_16x16x32_bf16 v[84:87], v[132:135], v[148:151], v[84:87]\n"\
  "ds_read_b128 v[172:175], %[aq] offset:4608\n"\
  "v_add_u32 %[aq], %[ta], %[aq]\n"\
  "v_mfma_f32_16x16x32_bf16 v[88:91], v[136:139], v[148:151], v[88:91]\n"\
  "ds_read_b64_tr_b16 v[180:181], %[t1] offset:8192\n"\
  "v_mfma_f32_16x16x32_bf16 v[92:95], v[140:143], v[148:151], v[92:95]\n"\
  "ds_read_b64_tr_b16 v[182:183], %[t1] offset:9216\n"\
  "v_add_u32 %[t1], %[ta], %[t1]\n"\
  "v_mfma_f32_16x16x32_bf16 v[96:99], v[128:131], v[152:155], v[96:99]\n"\
  "ds_read_b64_tr_b16 v[184:185], %[t2] offset:8192\n"\
  "v_mfma_f32_16x16x32_bf16 v[100:103], v[132:135], v[152:155], v[100:103]\n"\
  "ds_read_b64_tr_b16 v[186:187], %[t2] offset:9216\n"\
  "v_add_u32 %[t2], %[ta], %[t2]\n"\
  "v_mfma_f32_16x16x32_bf16 v[104:107], v[136:139], v[152:155], v[104:107]\n"\
  "ds_read_b64_tr_b16 v[188:189], %[t3] offset:8192\n"\
  "v_mfma_f32_16x16x32_bf16 v[108:111], v[140:143], v[152:155], v[108:111]\n"\
  "ds_read_b64_tr_b16 v[190:191], %[t3] offset:9216\n"\
  "s_waitcnt lgkmcnt(12)\n"\
  "v_mfma_f32_16x16x32_bf16 v[112:115], v[128:131], v[156:159], v[112:115]\n"\
  "v_add_u32 %[t3], %[ta], %[t3]\n"\
  "v_mfma_f32_16x16x32_bf16 v[116:119], v[132:135], v[156:159], v[116:119]\n"\
  "v_mfma_f32_16x16x32_bf16 v[120:123], v[136:139], v[156:159], v[120:123]\n"\
  "s_waitcnt lgkmcnt(0)\n"\
  "v_mfma_f32_16x16x32_bf16 v[124:127], v[140:143], v[156:159], v[124:127]\n"\
  "s_cbranch_scc0 1b\n"\
  "s_waitcnt lgkmcnt(0)\n"\
  "s_barrier\n"\
  "v_mfma_f32_16x16x32_bf16 v[64:67], v[160:163], v[176:179], v[64:67]\n"\
  "v_mfma_f32_16x16x32_bf16 v[68:71], v[164:167], v[176:179], v[68:71]\n"\
  "v_mfma_f32_16x16x32_bf16 v[72:75], v[168:171], v[176:179], v[72:75]\n"\
  "v_mfma_f32_16x16x32_bf16 v[76:79], v[172:175], v[176:179], v[76:79]\n"\
  "v_mfma_f32_16x16x32_bf16 v[80:83], v[160:163], v[180:183], v[80:83]\n"\
  "v_mfma_f32_16x16x32_bf16 v[84:87], v[164:167], v[180:183], v[84:87]\n"\
  "v_mfma_f32_16x16x32_bf16 v[88:91], v[168:171], v[180:183], v[88:91]\n"\
  "v_mfma_f32_16x16x32_bf16 v[92:95], v[172:175], v[180:183], v[92:95]\n"\
  "v_mfma_f32_16x16x32_bf16 v[96:99], v[160:163], v[184:187], v[96:99]\n"\
  "v_mfma_f32_16x16x32_bf16 v[100:103], v[164:167], v[184:187], v[100:103]\n"\
  "v_mfma_f32_16x16x32_bf16 v[104:107], v[168:171], v[184:187], v[104:107]\n"\
  "v_mfma_f32_16x16x32_bf16 v[108:111], v[172:175], v[184:187], v[108:111]\n"\
  "v_mfma_f32_16x16x32_bf16 v[112:115], v[160:163], v[188:191], v[112:115]\n"\
  "v_mfma_f32_16x16x32_bf16 v[116:119], v[164:167], v[188:191], v[116:119]\n"\
  "v_mfma_f32_16x16x32_bf16 v[120:123], v[168:171], v[188:191], v[120:123]\n"\
  "v_mfma_f32_16x16x32_bf16 v[124:127], v[172:175], v[188:191], v[124:127]\n"\
  "s_nop 7\n"\
  ""
#define GQ_CLOB "v128","v129","v130","v131","v132","v133","v134","v135","v136","v137","v138","v139","v140","v141","v142","v143","v144","v145","v146","v147","v148","v149","v150","v151","v152","v153","v154","v155","v156","v157","v158","v159","v160","v161","v162","v163","v164","v165","v166","v167","v168","v169","v170","v171","v172","v173","v174","v175","v176","v177","v178","v179","v180","v181","v182","v183","v184","v185","v186","v187","v188","v189","v190","v191"
#define PJ_ASM \
  "ds_read_b64_tr_b16 v[64:65], %[t0] offset:0\n"\
  "ds_read_b64_tr_b16 v[66:67], %[t0] offset:3072\n"\
  "ds_read_b64_tr_b16 v[68:69], %[t1] offset:0\n"\
  "ds_read_b64_tr_b16 v[70:71], %[t1] offset:3072\n"\
  "ds_read_b64_tr_b16 v[72:73], %[t2] offset:0\n"\
  "ds_read_b64_tr_b16 v[74:75], %[t2] offset:3072\n"\
  "ds_read_b64_tr_b16 v[76:77], %[t0] offset:6144\n"\
  "ds_read_b64_tr_b16 v[78:79], %[t0] offset:9216\n"\
  "ds_read_b64_tr_b16 v[80:81], %[t1] offset:6144\n"\
  "ds_read_b64_tr_b16 v[82:83], %[t1] offset:9216\n"\
  "ds_read_b64_tr_b16 v[84:85], %[t2] offset:6144\n"\
  "ds_read_b64_tr_b16 v[86:87], %[t2] offset:9216\n"\
  "ds_read_b64_tr_b16 v[88:89], %[t0] offset:12288\n"\
  "ds_read_b64_tr_b16 v[90:91], %[t0] offset:15360\n"\
  "ds_read_b64_tr_b16 v[92:93], %[t1] offset:12288\n"\
  "ds_read_b64_tr_b16 v[94:95], %[t1] offset:15360\n"\
  "ds_read_b64_tr_b16 v[96:97], %[t2] offset:12288\n"\
  "ds_read_b64_tr_b16 v[98:99], %[t2] offset:15360\n"\
  "ds_read_b64_tr_b16 v[100:101], %[t0] offset:18432\n"\
  "ds_read_b64_tr_b16 v[102:103], %[t0] offset:21504\n"\
  "ds_read_b64_tr_b16 v[104:105], %[t1] offset:18432\n"\
  "ds_read_b64_tr_b16 v[106:107], %[t1] offset:21504\n"\
  "ds_read_b64_tr_b16 v[108:109], %[t2] offset:18432\n"\
  "ds_read_b64_tr_b16 v[110:111], %[t2] offset:21504\n"\
  "ds_read_b64_tr_b16 v[112:113], %[t0] offset:24576\n"\
  "ds_read_b64_tr_b16 v[114:115], %[t0] offset:27648\n"\
  "ds_read_b64_tr_b16 v[116:117], %[t1] offset:24576\n"\
  "ds_read_b64_tr_b16 v[118:119], %[t1] offset:27648\n"\
  "ds_read_b64_tr_b16 v[120:121], %[t2] offset:24576\n"\
  "ds_read_b64_tr_b16 v[122:123], %[t2] offset:27648\n"\
  "ds_read_b64_tr_b16 v[124:125], %[t0] offset:30720\n"\
  "ds_read_b64_tr_b16 v[126:127], %[t0] offset:33792\n"\
  "ds_read_b64_tr_b16 v[128:129], %[t1] offset:30720\n"\
  "ds_read_b64_tr_b16 v[130:131], %[t1] offset:33792\n"\
  "ds_read_b64_tr_b16 v[132:133], %[t2] offset:30720\n"\
  "ds_read_b64_tr_b16 v[134:135], %[t2] offset:33792\n"\
  "ds_read_b64_tr_b16 v[136:137], %[t0] offset:36864\n"\
  "ds_read_b64_tr_b16 v[138:139], %[t0] offset:39936\n"\
  "ds_read_b64_tr_b16 v[140:141], %[t1] offset:36864\n"\
  "ds_read_b64_tr_b16 v[142:143], %[t1] offset:39936\n"\
  "ds_read_b64_tr_b16 v[144:145], %[t2] offset:36864\n"\
  "ds_read_b64_tr_b16 v[146:147], %[t2] offset:39936\n"\
  "ds_read_b64_tr_b16 v[148:149], %[t0] offset:43008\n"\
  "ds_read_b64_tr_b16 v[150:151], %[t0] offset:46080\n"\
  "ds_read_b64_tr_b16 v[152:153], %[t1] offset:43008\n"\
  "ds_read_b64_tr_b16 v[154:155], %[t1] offset:46080\n"\
  "ds_read_b64_tr_b16 v[156:157], %[t2] offset:43008\n"\
  "ds_read_b64_tr_b16 v[158:159], %[t2] offset:46080\n"\
  "ds_read_b64_tr_b16 v[160:161], %[t0] offset:49152\n"\
  "ds_read_b64_tr_b16 v[162:163], %[t0] offset:52224\n"\
  "ds_read_b64_tr_b16 v[164:165], %[t1] offset:49152\n"\
  "ds_read_b64_tr_b16 v[166:167], %[t1] offset:52224\n"\
  "ds_read_b64_tr_b16 v[168:169], %[t2] offset:49152\n"\
  "ds_read_b64_tr_b16 v[170:171], %[t2] offset:52224\n"\
  "ds_read_b64_tr_b16 v[172:173], %[t0] offset:55296\n"\
  "ds_read_b64_tr_b16 v[174:175], %[t0] offset:58368\n"\
  "ds_read_b64_tr_b16 v[176:177], %[t1] offset:55296\n"\
  "ds_read_b64_tr_b16 v[178:179], %[t1] offset:58368\n"\
  "ds_read_b64_tr_b16 v[180:181], %[t2] offset:55296\n"\
  "ds_read_b64_tr_b16 v[182:183], %[t2] offset:58368\n"\
  "ds_read_b64_tr_b16 v[184:185], %[u0] offset:0\n"\
  "ds_read_b64_tr_b16 v[186:187], %[u0] offset:3072\n"\
  "ds_read_b64_tr_b16 v[188:189], %[u1] offset:0\n"\
  "ds_read_b64_tr_b16 v[190:191], %[u1] offset:3072\n"\
  "ds_read_b64_tr_b16 v[192:193], %[u2] offset:0\n"\
  "ds_read_b64_tr_b16 v[194:195], %[u2] offset:3072\n"\
  "ds_read_b64_tr_b16 v[196:197], %[u0] offset:6144\n"\
  "ds_read_b64_tr_b16 v[198:199], %[u0] offset:9216\n"\
  "ds_read_b64_tr_b16 v[200:201], %[u1] offset:6144\n"\
  "ds_read_b64_tr_b16 v[202:203], %[u1] offset:9216\n"\
  "ds_read_b64_tr_b16 v[204:205], %[u2] offset:6144\n"\
  "ds_read_b64_tr_b16 v[206:207], %[u2] offset:9216\n"\
  "s_waitcnt lgkmcnt(0)\n"\
  "s_barrier\n"\
  "s_barrier\n"\
  "ds_read_b128 v[208:211], %[ap] offset:0\n"\
  "ds_read_b128 v[212:215], %[ap] offset:16384\n"\
  "ds_read_b128 v[216:219], %[ap] offset:1024\n"\
  "ds_read_b128 v[220:223], %[ap] offset:17408\n"\
  "s_waitcnt lgkmcnt(2)\n"\
  "v_mfma_f32_16x16x32_bf16 v[40:43], v[208:211], v[64:67], v[40:43]\n"\
  "ds_read_b128 v[224:227], %[ap] offset:2048\n"\
  "v_mfma_f32_16x16x32_bf16 v[44:47], v[208:211], v[68:71], v[44:47]\n"\
  "ds_read_b128 v[228:231], %[ap] offset:18432\n"\
  "v_mfma_f32_16x16x32_bf16 v[48:51], v[208:211], v[72:75], v[48:51]\n"\
  "v_mfma_f32_16x16x32_bf16 v[52:55], v[212:215], v[64:67], v[52:55]\n"\
  "v_mfma_f32_16x16x32_bf16 v[56:59], v[212:215], v[68:71], v[56:59]\n"\
  "v_mfma_f32_16x16x32_bf16 v[60:63], v[212:215], v[72:75], v[60:63]\n"\
  "s_waitcnt lgkmcnt(2)\n"\
  "v_mfma_f32_16x16x32_bf16 v[40:43], v[216:219], v[76:79], v[40:43]\n"\
  "ds_read_b128 v[208:211], %[ap] offset:3072\n"\
  "ds_read_b128 v[212:215], %[ap] offset:19456\n"\
  "v_mfma_f32_16x16x32_bf16 v[44:47], v[216:219], v[80:83], v[44:47]\n"\
  "ds_read_b64_tr_b16 v[64:65], %[u0] offset:12288\n"\
  "ds_read_b64_tr_b16 v[66:67], %[u0] offset:15360\n"\
  "v_mfma_f32_16x16x32_bf16 v[48:51], v[216:219], v[84:87], v[48:51]\n"\
  "ds_read_b64_tr_b16 v[68:69], %[u1] offset:12288\n"\
  "v_mfma_f32_16x16x32_bf16 v[52:55], v[220:223], v[76:79], v[52:55]\n"\
  "ds_read_b64_tr_b16 v[70:71], %[u1] offset:15360\n"\
  "v_mfma_f32_16x16x32_bf16 v[56:59], v[220:223], v[80:83], v[56:59]\n"\
  "ds_read_b64_tr_b16 v[72:73], %[u2] offset:12288\n"\
  "v_mfma_f32_16x16x32_bf16 v[60:63], v[220:223], v[84:87], v[60:63]\n"\
  "ds_read_b64_tr_b16 v[74:75], %[u2] offset:15360\n"\
  "s_waitcnt lgkmcnt(8)\n"\
  "v_mfma_f32_16x16x32_bf16 v[40:43], v[224:227], v[88:91], v[40:43]\n"\
  "s_barrier\n"\
  "ds_read_b128 v[216:219], %[ap] offset:4096\n"\
  "v_mfma_f32_16x16x32_bf16 v[44:47], v[224:227], v[92:95], v[44:47]\n"\
  "ds_read_b128 v[220:223], %[ap] offset:20480\n"\
  "ds_read_b64_tr_b16 v[76:77], %[u0] offset:18432\n"\
  "v_mfma_f32_16x16x32_bf16 v[48:51], v[224:227], v[96:99], v[48:51]\n"\
  "ds_read_b64_tr_b16 v[78:79], %[u0] offset:21504\n"\
  "ds_read_b64_tr_b16 v[80:81], %[u1] offset:18432\n"\
  "v_mfma_f32_16x16x32_bf16 v[52:55], v[228:231], v[88:91], v[52:55]\n"\
  "ds_read_b64_tr_b16 v[82:83], %[u1] offset:21504\n"\
  "v_mfma_f32_16x16x32_bf16 v[56:59], v[228:231], v[92:95], v[56:59]\n"\
  "ds_read_b64_tr_b16 v[84:85], %[u2] offset:18432\n"\
  "v_mfma_f32_16x16x32_bf16 v[60:63], v[228:231], v[96:99], v[60:63]\n"\
  "ds_read_b64_tr_b16 v[86:87], %[u2] offset:21504\n"\
  "s_waitcnt lgkmcnt(14)\n"\
  "v_mfma_f32_16x16x32_bf16 v[40:43], v[208:211], v[100:103], v[40:43]\n"\
  "ds_read_b128 v[224:227], %[ap] offset:5120\n"\
  "ds_read_b128 v[228:231], %[ap] offset:21504\n"\
  "v_mfma_f32_16x16x32_bf16 v[44:47], v[208:211], v[104:107], v[44:47]\n"\
  "ds_read_b64_tr_b16 v[88:89], %[u0] offset:24576\n"\
  "ds_read_b64_tr_b16 v[90:91], %[u0] offset:27648\n"\
  "v_mfma_f32_16x16x32_bf16 v[48:51], v[208:211], v[108:111], v[48:51]\n"\
  "ds_read_b64_tr_b16 v[92:93], %[u1] offset:24576\n"\
  "v_mfma_f32_16x16x32_bf16 v[52:55], v[212:215], v[100:103], v[52:55]\n"\
  "ds_read_b64_tr_b16 v[94:95], %[u1] offset:27648\n"\
  "v_mfma_f32_16x16x32_bf16 v[56:59], v[212:215], v[104:107], v[56:59]\n"\
  "ds_read_b64_tr_b16 v[96:97], %[u2] offset:24576\n"\
  "v_mfma_f32_16x16x32_bf16 v[60:63], v[212:215], v[108:111], v[60:63]\n"\
  "ds_read_b64_tr_b16 v[98:99], %[u2] offset:27648\n"\
  "s_waitcnt lgkmcnt(14)\n"\
  "v_mfma_f32_16x16x32_bf16 v[40:43], v[216:219], v[112:115], v[40:43]\n"\
  "ds_read_b128 v[208:211], %[ap] offset:6144\n"\
  "ds_read_b128 v[212:215], %[ap] offset:22528\n"\
  "v_mfma_f32_16x16x32_bf16 v[44:47], v[216:219], v[116:119], v[44:47]\n"\
  "ds_read_b64_tr_b16 v[100:101], %[u0] offset:30720\n"\
  "ds_read_b64_tr_b16 v[102:103], %[u0] offset:33792\n"\
  "v_mfma_f32_16x16x32_bf16 v[48:51], v[216:219], v[120:123], v[48:51]\n"\
  "ds_read_b64_tr_b16 v[104:105], %[u1] offset:30720\n"\
  "v_mfma_f32_16x16x32_bf16 v[52:55], v[220:223], v[112:115], v[52:55]\n"\
  "ds_read_b64_tr_b16 v[106:107], %[u1] offset:33792\n"\
  "v_mfma_f32_16x16x32_bf16 v[56:59], v[220:223], v[116:119], v[56:59]\n"\
  "ds_read_b64_tr_b16 v[108:109], %[u2] offset:30720\n"\
  "v_mfma_f32_16x16x32_bf16 v[60:63], v[220:223], v[120:123], v[60:63]\n"\
  "ds_read_b64_tr_b16 v[110:111], %[u2] offset:33792\n"\
  "s_waitcnt lgkmcnt(14)\n"\
  "v_mfma_f32_16x16x32_bf16 v[40:43], v[224:227], v[124:127], v[40:43]\n"\
  "ds_read_b128 v[216:219], %[ap] offset:7168\n"\
  "ds_read_b128 v[220:223], %[ap] offset:23552\n"\
  "v_mfma_f32_16x16x32_bf16 v[44:47], v[224:227], v[128:131], v[44:47]\n"\
  "ds_read_b64_tr_b16 v[112:113], %[u0] offset:36864\n"\
  "ds_read_b64_tr_b16 v[114:115], %[u0] offset:39936\n"\
  "v_mfma_f32_16x16x32_bf16 v[48:51], v[224:227], v[132:135], v[48:51]\n"\
  "ds_read_b64_tr_b16 v[116:117], %[u1] offset:36864\n"\
  "v_mfma_f32_16x16x32_bf16 v[52:55], v[228:231], v[124:127], v[52:55]\n"\
  "ds_read_b64_tr_b16 v[118:119], %[u1] offset:39936\n"\
  "v_mfma_f32_16x16x32_bf16 v[56:59], v[228:231], v[128:131], v[56:59]\n"\
  "ds_read_b64_tr_b16 v[120:121], %[u2] offset:36864\n"\
  "v_mfma_f32_16x16x32_bf16 v[60:63], v[228:231], v[132:135], v[60:63]\n"\
  "ds_read_b64_tr_b16 v[122:123], %[u2] offset:39936\n"\
  "s_waitcnt lgkmcnt(14)\n"\
  "v_mfma_f32_16x16x32_bf16 v[40:43], v[208:211], v[136:139], v[40:43]\n"\
  "s_barrier\n"\
  "ds_read_b128 v[224:227], %[ap] offset:8192\n"\
  "v_mfma_f32_16x16x32_bf16 v[44:47], v[208:211], v[140:143], v[44:47]\n"\
  "ds_read_b128 v[228:231], %[ap] offset:24576\n"\
  "ds_read_b64_tr_b16 v[124:125], %[u0] offset:43008\n"\
  "v_mfma_f32_16x16x32_bf16 v[48:51], v[208:211], v[144:147], v[48:51]\n"\
  "ds_read_b64_tr_b16 v[126:127], %[u0] offset:46080\n"\
  "ds_read_b64_tr_b16 v[128:129], %[u1] offset:43008\n"\
  "v_mfma_f32_16x16x32_bf16 v[52:55], v[212:215], v[136:139], v[52:55]\n"\
  "ds_read_b64_tr_b16 v[130:131], %[u1] offset:46080\n"\
  "v_mfma_f32_16x16x32_bf16 v[56:59], v[212:215], v[140:143], v[56:59]\n"\
  "ds_read_b64_tr_b16 v[132:133], %[u2] offset:43008\n"\
  "v_mfma_f32_16x16x32_bf16 v[60:63], v[212:215], v[144:147], v[60:63]\n"\
  "ds_read_b64_tr_b16 v[134:135], %[u2] offset:46080\n"\
  "s_waitcnt lgkmcnt(14)\n"\
  "v_mfma_f32_16x16x32_bf16 v[40:43], v[216:219], v[148:151], v[40:43]\n"\
  "ds_read_b128 v[208:211], %[ap] offset:9216\n"\
  "v_mfma_f32_16x16x32_bf16 v[44:47], v[216:219], v[152:155], v[44:47]\n"\
  "ds_read_b128 v[212:215], %[ap] offset:25600\n"\
  "v_mfma_f32_16x16x32_bf16 v[48:51], v[216:219], v[156:159], v[48:51]\n"\
  "v_mfma_f32_16x16x32_bf16 v[52:55], v[220:223], v[148:151], v[52:55]\n"\
  "v_mfma_f32_16x16x32_bf16 v[56:59], v[220:223], v[152:155], v[56:59]\n"\
  "v_mfma_f32_16x16x32_bf16 v[60:63], v[220:223], v[156:159], v[60:63]\n"\
  "s_waitcnt lgkmcnt(8)\n"\
  "v_mfma_f32_16x16x32_bf16 v[40:43], v[224:227], v[160:163], v[40:43]\n"\
  "ds_read_b128 v[216:219], %[ap] offset:10240\n"\
  "v_mfma_f32_16x16x32_bf16 v[44:47], v[224:227], v[164:167], v[44:47]\n"\
  "ds_read_b128 v[220:223], %[ap] offset:26624\n"\
  "v_mfma_f32_16x16x32_bf16 v[48:51], v[224:227], v[168:171], v[48:51]\n"\
  "v_mfma_f32_16x16x32_bf16 v[52:55], v[228:231], v[160:163], v[52:55]\n"\
  "v_mfma_f32_16x16x32_bf16 v[56:59], v[228:231], v[164:167], v[56:59]\n"\
  "v_mfma_f32_16x16x32_bf16 v[60:63], v[228:231], v[168:171], v[60:63]\n"\
  "s_waitcnt lgkmcnt(2)\n"\
  "v_mfma_f32_16x16x32_bf16 v[40:43], v[208:211], v[172:175], v[40:43]\n"\
  "ds_read_b128 v[224:227], %[ap] offset:11264\n"\
  "v_mfma_f32_16x16x32_bf16 v[44:47], v[208:211], v[176:179], v[44:47]\n"\
  "ds_read_b128 v[228:231], %[ap] offset:27648\n"\
  "v_mfma_f32_16x16x32_bf16 v[48:51], v[208:211], v[180:183], v[48:51]\n"\
  "v_mfma_f32_16x16x32_bf16 v[52:55], v[212:215], v[172:175], v[52:55]\n"\
  "v_mfma_f32_16x16x32_bf16 v[56:59], v[212:215], v[176:179], v[56:59]\n"\
  "v_mfma_f32_16x16x32_bf16 v[60:63], v[212:215], v[180:183], v[60:63]\n"\
  "s_waitcnt lgkmcnt(2)\n"\
  "v_mfma_f32_16x16x32_bf16 v[40:43], v[216:219], v[184:187], v[40:43]\n"\
  "ds_read_b128 v[208:211], %[ap] offset:12288\n"\
  "v_mfma_f32_16x16x32_bf16 v[44:47], v[216:219], v[188:191], v[44:47]\n"\
  "ds_read_b128 v[212:215], %[ap] offset:28672\n"\
  "v_mfma_f32_16x16x32_bf16 v[48:51], v[216:219], v[192:195], v[48:51]\n"\
  "v_mfma_f32_16x16x32_bf16 v[52:55], v[220:223], v[184:187], v[52:55]\n"\
  "v_mfma_f32_16x16x32_bf16 v[56:59], v[220:223], v[188:191], v[56:59]\n"\
  "v_mfma_f32_16x16x32_bf16 v[60:63], v[220:223], v[192:195], v[60:63]\n"\
  "s_waitcnt lgkmcnt(2)\n"\
  "v_mfma_f32_16x16x32_bf16 v[40:43], v[224:227], v[196:199], v[40:43]\n"\
  "s_barrier\n"\
  "v_mfma_f32_16x16x32_bf16 v[44:47], v[224:227], v[200:203], v[44:47]\n"\
  "ds_read_b128 v[216:219], %[ap] offset:13312\n"\
  "v_mfma_f32_16x16x32_bf16 v[48:51], v[224:227], v[204:207], v[48:51]\n"\
  "ds_read_b128 v[220:223], %[ap] offset:29696\n"\
  "v_mfma_f32_16x16x32_bf16 v[52:55], v[228:231], v[196:199], v[52:55]\n"\
  "v_mfma_f32_16x16x32_bf16 v[56:59], v[228:231], v[200:203], v[56:59]\n"\
  "v_mfma_f32_16x16x32_bf16 v[60:63], v[228:231], v[204:207], v[60:63]\n"\
  "s_waitcnt lgkmcnt(2)\n"\
  "v_mfma_f32_16x16x32_bf16 v[40:43], v[208:211], v[64:67], v[40:43]\n"\
  "ds_read_b128 v[224:227], %[ap] offset:14336\n"\
  "v_mfma_f32_16x16x32_bf16 v[44:47], v[208:211], v[68:71], v[44:47]\n"\
  "ds_read_b128 v[228:231], %[ap] offset:30720\n"\
  "v_mfma_f32_16x16x32_bf16 v[48:51], v[208:211], v[72:75], v[48:51]\n"\
  "v_mfma_f32_16x16x32_bf16 v[52:55], v[212:215], v[64:67], v[52:55]\n"\
  "v_mfma_f32_16x16x32_bf16 v[56:59], v[212:215], v[68:71], v[56:59]\n"\
  "v_mfma_f32_16x16x32_bf16 v[60:63], v[212:215], v[72:75], v[60:63]\n"\
  "s_waitcnt lgkmcnt(2)\n"\
  "v_mfma_f32_16x16x32_bf16 v[40:43], v[216:219], v[76:79], v[40:43]\n"\
  "ds_read_b128 v[208:211], %[ap] offset:15360\n"\
  "v_mfma_f32_16x16x32_bf16 v[44:47], v[216:219], v[80:83], v[44:47]\n"\
  "ds_read_b128 v[212:215], %[ap] offset:31744\n"\
  "v_mfma_f32_16x16x32_bf16 v[48:51], v[216:219], v[84:87], v[48:51]\n"\
  "v_mfma_f32_16x16x32_bf16 v[52:55], v[220:223], v[76:79], v[52:55]\n"\
  "v_mfma_f32_16x16x32_bf16 v[56:59], v[220:223], v[80:83], v[56:59]\n"\
  "v_mfma_f32_16x16x32_bf16 v[60:63], v[220:223], v[84:87], v[60:63]\n"\
  "s_waitcnt lgkmcnt(2)\n"\
  "v_mfma_f32_16x16x32_bf16 v[40:43], v[224:227], v[88:91], v[40:43]\n"\
  "ds_read_b32 v216, %[at] offset:0\n"\
  "ds_read_b32 v217, %[at] offset:256\n"\
  "v_mfma_f32_16x16x32_bf16 v[44:47], v[224:227], v[92:95], v[44:47]\n"\
  "ds_read_b32 v218, %[at] offset:512\n"\
  "ds_read_b32 v219, %[at] offset:768\n"\
  "v_mfma_f32_16x16x32_bf16 v[48:51], v[224:227], v[96:99], v[48:51]\n"\
  "ds_read_b32 v220, %[at] offset:64\n"\
  "v_mfma_f32_16x16x32_bf16 v[52:55], v[228:231], v[88:91], v[52:55]\n"\
  "ds_read_b32 v221, %[at] offset:320\n"\
  "v_mfma_f32_16x16x32_bf16 v[56:59], v[228:231], v[92:95], v[56:59]\n"\
  "ds_read_b32 v222, %[at] offset:576\n"\
  "v_mfma_f32_16x16x32_bf16 v[60:63], v[228:231], v[96:99], v[60:63]\n"\
  "ds_read_b32 v223, %[at] offset:832\n"\
  "s_waitcnt lgkmcnt(8)\n"\
  "v_mfma_f32_16x16x32_bf16 v[40:43], v[208:211], v[100:103], v[40:43]\n"\
  "ds_read_b32 v224, %[at] offset:4096\n"\
  "ds_read_b32 v225, %[at] offset:4352\n"\
  "v_mfma_f32_16x16x32_bf16 v[44:47], v[208:211], v[104:107], v[44:47]\n"\
  "ds_read_b32 v226, %[at] offset:4608\n"\
  "ds_read_b32 v227, %[at] offset:4864\n"\
  "v_mfma_f32_16x16x32_bf16 v[48:51], v[208:211], v[108:111], v[48:51]\n"\
  "ds_read_b32 v228, %[at] offset:4160\n"\
  "v_mfma_f32_16x16x32_bf16 v[52:55], v[212:215], v[100:103], v[52:55]\n"\
  "ds_read_b32 v229, %[at] offset:4416\n"\
  "v_mfma_f32_16x16x32_bf16 v[56:59], v[212:215], v[104:107], v[56:59]\n"\
  "ds_read_b32 v230, %[at] offset:4672\n"\
  "v_mfma_f32_16x16x32_bf16 v[60:63], v[212:215], v[108:111], v[60:63]\n"\
  "ds_read_b32 v231, %[at] offset:4928\n"\
  "s_waitcnt lgkmcnt(8)\n"\
  "v_mfma_f32_16x16x32_bf16 v[40:43], v[216:219], v[112:115], v[40:43]\n"\
  "v_mfma_f32_16x16x32_bf16 v[44:47], v[216:219], v[116:119], v[44:47]\n"\
  "v_mfma_f32_16x16x32_bf16 v[48:51], v[216:219], v[120:123], v[48:51]\n"\
  "v_mfma_f32_16x16x32_bf16 v[52:55], v[220:223], v[112:115], v[52:55]\n"\
  "v_mfma_f32_16x16x32_bf16 v[56:59], v[220:223], v[116:119], v[56:59]\n"\
  "v_mfma_f32_16x16x32_bf16 v[60:63], v[220:223], v[120:123], v[60:63]\n"\
  "s_waitcnt lgkmcnt(0)\n"\
  "s_barrier\n"\
  "v_mfma_f32_16x16x32_bf16 v[40:43], v[224:227], v[124:127], v[40:43]\n"\
  "v_mfma_f32_16x16x32_bf16 v[44:47], v[224:227], v[128:131], v[44:47]\n"\
  "v_mfma_f32_16x16x32_bf16 v[48:51], v[224:227], v[132:135], v[48:51]\n"\
  "v_mfma_f32_16x16x32_bf16 v[52:55], v[228:231], v[124:127], v[52:55]\n"\
  "v_mfma_f32_16x16x32_bf16 v[56:59], v[228:231], v[128:131], v[56:59]\n"\
  "v_mfma_f32_16x16x32_bf16 v[60:63], v[228:231], v[132:135], v[60:63]\n"\
  "s_nop 7\n"\
  ""
#define PJ_CLOB "v64","v65","v66","v67","v68","v69","v70","v71","v72","v73","v74","v75","v76","v77","v78","v79","v80","v81","v82","v83","v84","v85","v86","v87","v88","v89","v90","v91","v92","v93","v94","v95","v96","v97","v98","v99","v100","v101","v102","v103","v104","v105","v106","v107","v108","v109","v110","v111","v112","v113","v114","v115","v116","v117","v118","v119","v120","v121","v122","v123","v124","v125","v126","v127","v128","v129","v130","v131","v132","v133","v134","v135","v136","v137","v138","v139","v140","v141","v142","v143","v144","v145","v146","v147","v148","v149","v150","v151","v152","v153","v154","v155","v156","v157","v158","v159","v160","v161","v162","v163","v164","v165","v166","v167","v168","v169","v170","v171","v172","v173","v174","v175","v176","v177","v178","v179","v180","v181","v182","v183","v184","v185","v186","v187","v188","v189","v190","v191","v192","v193","v194","v195","v196","v197","v198","v199","v200","v201","v202","v203","v204","v205","v206","v207","v208","v209","v210","v211","v212","v213","v214","v215","v216","v217","v218","v219","v220","v221","v222","v223","v224","v225","v226","v227","v228","v229","v230","v231"
#define G1_ASM \
  "v_mov_b32 v140, 0\n"\
  "v_mov_b32 v141, 0\n"\
  "v_mov_b32 v142, 0\n"\
  "v_mov_b32 v143, 0\n"\
  "v_mov_b32 v144, 0\n"\
  "v_mov_b32 v145, 0\n"\
  "v_mov_b32 v146, 0\n"\
  "v_mov_b32 v147, 0\n"\
  "v_mov_b32 v148, 0\n"\
  "v_mov_b32 v149, 0\n"\
  "v_mov_b32 v150, 0\n"\
  "v_mov_b32 v151, 0\n"\
  "v_mov_b32 v152, 0\n"\
  "v_mov_b32 v153, 0\n"\
  "v_mov_b32 v154, 0\n"\
  "v_mov_b32 v155, 0\n"\
  "v_mov_b32 v156, 0\n"\
  "v_mov_b32 v157, 0\n"\
  "v_mov_b32 v158, 0\n"\
  "v_mov_b32 v159, 0\n"\
  "v_mov_b32 v160, 0\n"\
  "v_mov_b32 v161, 0\n"\
  "v_mov_b32 v162, 0\n"\
  "v_mov_b32 v163, 0\n"\
  "v_mov_b32 v164, 0\n"\
  "v_mov_b32 v165, 0\n"\
  "v_mov_b32 v166, 0\n"\
  "v_mov_b32 v167, 0\n"\
  "1:\n"\
  "s_barrier\n"\
  "v_mfma_f32_16x16x32_bf16 v[64:67], v[152:155], v[140:143], v[64:67]\n"\
  "ds_read_b64_tr_b16 v[124:125], %[t0] offset:0\n"\
  "s_bitcmp1_b32 %[ma], %[cnt]\n"\
  "v_mfma_f32_16x16x32_bf16 v[68:71], v[152:155], v[144:147], v[68:71]\n"\
  "ds_read_b64_tr_b16 v[126:127], %[t0] offset:4096\n"\
  "s_cselect_b32 %[ta], %[wa], %[sa]\n"\
  "v_mfma_f32_16x16x32_bf16 v[72:75], v[152:155], v[148:151], v[72:75]\n"\
  "ds_read_b128 v[112:115], %[ap] offset:0\n"\
  "s_bitcmp1_b32 %[mb], %[cnt]\n"\
  "v_mfma_f32_16x16x32_bf16 v[76:79], v[156:159], v[140:143], v[76:79]\n"\
  "ds_read_b128 v[116:119], %[ap] offset:1024\n"\
  "s_cselect_b32 %[tb], %[wb], %[sb]\n"\
  "v_mfma_f32_16x16x32_bf16 v[80:83], v[156:159], v[144:147], v[80:83]\n"\
  "ds_read_b128 v[120:123], %[ap] offset:2048\n"\
  "s_sub_u32 %[cnt], %[cnt], 1\n"\
  "v_mfma_f32_16x16x32_bf16 v[84:87], v[156:159], v[148:151], v[84:87]\n"\
  "ds_read_b64_tr_b16 v[128:129], %[t1] offset:0\n"\
  "v_mfma_f32_16x16x32_bf16 v[88:91], v[160:163], v[140:143], v[88:91]\n"\
  "ds_read_b64_tr_b16 v[130:131], %[t1] offset:4096\n"\
  "v_mfma_f32_16x16x32_bf16 v[92:95], v[160:163], v[144:147], v[92:95]\n"\
  "ds_read_b64_tr_b16 v[132:133], %[t2] offset:0\n"\
  "v_mfma_f32_16x16x32_bf16 v[96:99], v[160:163], v[148:151], v[96:99]\n"\
  "ds_read_b64_tr_b16 v[134:135], %[t2] offset:4096\n"\
  "v_mfma_f32_16x16x32_bf16 v[100:103], v[164:167], v[140:143], v[100:103]\n"\
  "ds_read_b64_tr_b16 v[136:137], %[t3] offset:0\n"\
  "v_mfma_f32_16x16x32_bf16 v[104:107], v[164:167], v[144:147], v[104:107]\n"\
  "ds_read_b64_tr_b16 v[138:139], %[t3] offset:4096\n"\
  "v_mfma_f32_16x16x32_bf16 v[108:111], v[164:167], v[148:151], v[108:111]\n"\
  "s_waitcnt lgkmcnt(4)\n"\
  "v_mfma_f32_16x16x32_bf16 v[64:67], v[124:127], v[112:115], v[64:67]\n"\
  "ds_read_b64_tr_b16 v[152:153], %[t0] offset:8192\n"\
  "ds_read_b64_tr_b16 v[154:155], %[t0] offset:12288\n"\
  "v_mfma_f32_16x16x32_bf16 v[68:71], v[124:127], v[116:119], v[68:71]\n"\
  "ds_read_b128 v[140:143], %[ap] offset:6144\n"\
  "ds_read_b128 v[144:147], %[ap] offset:7168\n"\
  "v_mfma_f32_16x16x32_bf16 v[72:75], v[124:127], v[120:123], v[72:75]\n"\
  "ds_read_b128 v[148:151], %[ap] offset:8192\n"\
  "v_add_u32 %[t0], %[tb], %[t0]\n"\
  "v_mfma_f32_16x16x32_bf16 v[76:79], v[128:131], v[112:115], v[76:79]\n"\
  "ds_read_b64_tr_b16 v[156:157], %[t1] offset:8192\n"\
  "v_add_u32 %[ap], %[ta], %[ap]\n"\
  "v_mfma_f32_16x16x32_bf16 v[80:83], v[128:131], v[116:119], v[80:83]\n"\
  "ds_read_b64_tr_b16 v[158:159], %[t1] offset:12288\n"\
  "v_add_u32 %[t1], %[tb], %[t1]\n"\
  "v_mfma_f32_16x16x32_bf16 v[84:87], v[128:131], v[120:123], v[84:87]\n"\
  "ds_read_b64_tr_b16 v[160:161], %[t2] offset:8192\n"\
  "s_waitcnt lgkmcnt(8)\n"\
  "v_mfma_f32_16x16x32_bf16 v[88:91], v[132:135], v[112:115], v[88:91]\n"\
  "ds_read_b64_tr_b16 v[162:163], %[t2] offset:12288\n"\
  "v_add_u32 %[t2], %[tb], %[t2]\n"\
  "v_mfma_f32_16x16x32_bf16 v[92:95], v[132:135], v[116:119], v[92:95]\n"\
  "ds_read_b64_tr_b16 v[164:165], %[t3] offset:8192\n"\
  "v_mfma_f32_16x16x32_bf16 v[96:99], v[132:135], v[120:123], v[96:99]\n"\
  "ds_read_b64_tr_b16 v[166:167], %[t3] offset:12288\n"\
  "v_add_u32 %[t3], %[tb], %[t3]\n"\
  "v_mfma_f32_16x16x32_bf16 v[100:103], v[136:139], v[112:115], v[100:103]\n"\
  "v_mfma_f32_16x16x32_bf16 v[104:107], v[136:139], v[116:119], v[104:107]\n"\
  "s_waitcnt lgkmcnt(0)\n"\
  "v_mfma_f32_16x16x32_bf16 v[108:111], v[136:139], v[120:123], v[108:111]\n"\
  "s_cbranch_scc0 1b\n"\
  "s_waitcnt lgkmcnt(0)\n"\
  "s_barrier\n"\
  "v_mfma_f32_16x16x32_bf16 v[64:67], v[152:155], v[140:143], v[64:67]\n"\
  "v_mfma_f32_16x16x32_bf16 v[68:71], v[152:155], v[144:147], v[68:71]\n"\
  "v_mfma_f32_16x16x32_bf16 v[72:75], v[152:155], v[148:151], v[72:75]\n"\
  "v_mfma_f32_16x16x32_bf16 v[76:79], v[156:159], v[140:143], v[76:79]\n"\
  "v_mfma_f32_16x16x32_bf16 v[80:83], v[156:159], v[144:147], v[80:83]\n"\
  "v_mfma_f32_16x16x32_bf16 v[84:87], v[156:159], v[148:151], v[84:87]\n"\
  "v_mfma_f32_16x16x32_bf16 v[88:91], v[160:163], v[140:143], v[88:91]\n"\
  "v_mfma_f32_16x16x32_bf16 v[92:95], v[160:163], v[144:147], v[92:95]\n"\
  "v_mfma_f32_16x16x32_bf16 v[96:99], v[160:163], v[148:151], v[96:99]\n"\
  "v_mfma_f32_16x16x32_bf16 v[100:103], v[164:167], v[140:143], v[100:103]\n"\
  "v_mfma_f32_16x16x32_bf16 v[104:107], v[164:167], v[144:147], v[104:107]\n"\
  "v_mfma_f32_16x16x32_bf16 v[108:111], v[164:167], v[148:151], v[108:111]\n"\
  "s_nop 7\n"\
  ""
#define G1_CLOB "v112","v113","v114","v115","v116","v117","v118","v119","v120","v121","v122","v123","v124","v125","v126","v127","v128","v129","v130","v131","v132","v133","v134","v135","v136","v137","v138","v139","v140","v141","v142","v143","v144","v145","v146","v147","v148","v149","v150","v151","v152","v153","v154","v155","v156","v157","v158","v159","v160","v161","v162","v163","v164","v165","v166","v167"
#define G2_ASM \
  "v_mov_b32 v180, 0\n"\
  "v_mov_b32 v181, 0\n"\
  "v_mov_b32 v182, 0\n"\
  "v_mov_b32 v183, 0\n"\
  "v_mov_b32 v184, 0\n"\
  "v_mov_b32 v185, 0\n"\
  "v_mov_b32 v186, 0\n"\
  "v_mov_b32 v187, 0\n"\
  "v_mov_b32 v188, 0\n"\
  "v_mov_b32 v189, 0\n"\
  "v_mov_b32 v190, 0\n"\
  "v_mov_b32 v191, 0\n"\
  "v_mov_b32 v192, 0\n"\
  "v_mov_b32 v193, 0\n"\
  "v_mov_b32 v194, 0\n"\
  "v_mov_b32 v195, 0\n"\
  "v_mov_b32 v196, 0\n"\
  "v_mov_b32 v197, 0\n"\
  "v_mov_b32 v198, 0\n"\
  "v_mov_b32 v199, 0\n"\
  "v_mov_b32 v200, 0\n"\
  "v_mov_b32 v201, 0\n"\
  "v_mov_b32 v202, 0\n"\
  "v_mov_b32 v203, 0\n"\
  "v_mov_b32 v204, 0\n"\
  "v_mov_b32 v205, 0\n"\
  "v_mov_b32 v206, 0\n"\
  "v_mov_b32 v207, 0\n"\
  "v_mov_b32 v208, 0\n"\
  "v_mov_b32 v209, 0\n"\
  "v_mov_b32 v210, 0\n"\
  "v_mov_b32 v211, 0\n"\
  "v_mov_b32 v212, 0\n"\
  "v_mov_b32 v213, 0\n"\
  "v_mov_b32 v214, 0\n"\
  "v_mov_b32 v215, 0\n"\
  "v_mov_b32 v216, 0\n"\
  "v_mov_b32 v217, 0\n"\
  "v_mov_b32 v218, 0\n"\
  "v_mov_b32 v219, 0\n"\
  "v_mov_b32 v220, 0\n"\
  "v_mov_b32 v221, 0\n"\
  "v_mov_b32 v222, 0\n"\
  "v_mov_b32 v223, 0\n"\
  "1:\n"\
  "s_barrier\n"\
  "v_mfma_f32_16x16x32_bf16 v[64:67], v[216:219], v[180:183], v[64:67]\n"\
  "ds_read_b64_tr_b16 v[172:173], %[t0] offset:0\n"\
  "s_bitcmp1_b32 %[ma], %[cnt]\n"\
  "v_mfma_f32_16x16x32_bf16 v[100:103], v[220:223], v[180:183], v[100:103]\n"\
  "ds_read_b64_tr_b16 v[174:175], %[t0] offset:4096\n"\
  "s_cselect_b32 %[ta], %[wa], %[sa]\n"\
  "v_mfma_f32_16x16x32_bf16 v[68:71], v[216:219], v[184:187], v[68:71]\n"\
  "ds_read_b128 v[136:139], %[ap] offset:0\n"\
  "s_bitcmp1_b32 %[mb], %[cnt]\n"\
  "v_mfma_f32_16x16x32_bf16 v[104:107], v[220:223], v[184:187], v[104:107]\n"\
  "ds_read_b64_tr_b16 v[176:177], %[t1] offset:0\n"\
  "s_cselect_b32 %[tb], %[wb], %[sb]\n"\
  "v_mfma_f32_16x16x32_bf16 v[72:75], v[216:219], v[188:191], v[72:75]\n"\
  "ds_read_b64_tr_b16 v[178:179], %[t1] offset:4096\n"\
  "s_sub_u32 %[cnt], %[cnt], 1\n"\
  "v_mfma_f32_16x16x32_bf16 v[108:111], v[220:223], v[188:191], v[108:111]\n"\
  "ds_read_b128 v[140:143], %[ap] offset:1024\n"\
  "v_mfma_f32_16x16x32_bf16 v[76:79], v[216:219], v[192:195], v[76:79]\n"\
  "ds_read_b128 v[144:147], %[ap] offset:2048\n"\
  "v_mfma_f32_16x16x32_bf16 v[112:115], v[220:223], v[192:195], v[112:115]\n"\
  "ds_read_b128 v[148:151], %[ap] offset:3072\n"\
  "v_mfma_f32_16x16x32_bf16 v[80:83], v[216:219], v[196:199], v[80:83]\n"\
  "ds_read_b128 v[152:155], %[ap] offset:4096\n"\
  "v_mfma_f32_16x16x32_bf16 v[116:119], v[220:223], v[196:199], v[116:119]\n"\
  "ds_read_b128 v[156:159], %[ap] offset:5120\n"\
  "v_mfma_f32_16x16x32_bf16 v[84:87], v[216:219], v[200:203], v[84:87]\n"\
  "ds_read_b128 v[160:163], %[ap] offset:6144\n"\
  "v_mfma_f32_16x16x32_bf16 v[120:123], v[220:223], v[200:203], v[120:123]\n"\
  "ds_read_b128 v[164:167], %[ap] offset:7168\n"\
  "v_mfma_f32_16x16x32_bf16 v[88:91], v[216:219], v[204:207], v[88:91]\n"\
  "ds_read_b128 v[168:171], %[ap] offset:8192\n"\
  "v_mfma_f32_16x16x32_bf16 v[124:127], v[220:223], v[204:207], v[124:127]\n"\
  "v_mfma_f32_16x16x32_bf16 v[92:95], v[216:219], v[208:211], v[92:95]\n"\
  "v_mfma_f32_16x16x32_bf16 v[128:131], v[220:223], v[208:211], v[128:131]\n"\
  "v_mfma_f32_16x16x32_bf16 v[96:99], v[216:219], v[212:215], v[96:99]\n"\
  "v_mfma_f32_16x16x32_bf16 v[132:135], v[220:223], v[212:215], v[132:135]\n"\
  "s_waitcnt lgkmcnt(0)\n"\
  "v_mfma_f32_16x16x32_bf16 v[64:67], v[172:175], v[136:139], v[64:67]\n"\
  "ds_read_b64_tr_b16 v[216:217], %[t0] offset:8192\n"\
  "v_mfma_f32_16x16x32_bf16 v[100:103], v[176:179], v[136:139], v[100:103]\n"\
  "ds_read_b64_tr_b16 v[218:219], %[t0] offset:12288\n"\
  "v_add_u32 %[t0], %[tb], %[t0]\n"\
  "v_mfma_f32_16x16x32_bf16 v[68:71], v[172:175], v[140:143], v[68:71]\n"\
  "ds_read_b128 v[180:183], %[ap] offset:9216\n"\
  "v_mfma_f32_16x16x32_bf16 v[104:107], v[176:179], v[140:143], v[104:107]\n"\
  "ds_read_b64_tr_b16 v[220:221], %[t1] offset:8192\n"\
  "v_mfma_f32_16x16x32_bf16 v[72:75], v[172:175], v[144:147], v[72:75]\n"\
  "ds_read_b64_tr_b16 v[222:223], %[t1] offset:12288\n"\
  "v_add_u32 %[t1], %[tb], %[t1]\n"\
  "v_mfma_f32_16x16x32_bf16 v[108:111], v[176:179], v[144:147], v[108:111]\n"\
  "ds_read_b128 v[184:187], %[ap] offset:10240\n"\
  "v_mfma_f32_16x16x32_bf16 v[76:79], v[172:175], v[148:151], v[76:79]\n"\
  "ds_read_b128 v[188:191], %[ap] offset:11264\n"\
  "v_mfma_f32_16x16x32_bf16 v[112:115], v[176:179], v[148:151], v[112:115]\n"\
  "ds_read_b128 v[192:195], %[ap] offset:12288\n"\
  "v_mfma_f32_16x16x32_bf16 v[80:83], v[172:175], v[152:155], v[80:83]\n"\
  "ds_read_b128 v[196:199], %[ap] offset:13312\n"\
  "v_mfma_f32_16x16x32_bf16 v[116:119], v[176:179], v[152:155], v[116:119]\n"\
  "ds_read_b128 v[200:203], %[ap] offset:14336\n"\
  "v_mfma_f32_16x16x32_bf16 v[84:87], v[172:175], v[156:159], v[84:87]\n"\
  "ds_read_b128 v[204:207], %[ap] offset:15360\n"\
  "v_mfma_f32_16x16x32_bf16 v[120:123], v[176:179], v[156:159], v[120:123]\n"\
  "ds_read_b128 v[208:211], %[ap] offset:16384\n"\
  "v_mfma_f32_16x16x32_bf16 v[88:91], v[172:175], v[160:163], v[88:91]\n"\
  "ds_read_b128 v[212:215], %[ap] offset:17408\n"\
  "v_add_u32 %[ap], %[ta], %[ap]\n"\
  "v_mfma_f32_16x16x32_bf16 v[124:127], v[176:179], v[160:163], v[124:127]\n"\
  "v_mfma_f32_16x16x32_bf16 v[92:95], v[172:175], v[164:167], v[92:95]\n"\
  "v_mfma_f32_16x16x32_bf16 v[128:131], v[176:179], v[164:167], v[128:131]\n"\
  "v_mfma_f32_16x16x32_bf16 v[96:99], v[172:175], v[168:171], v[96:99]\n"\
  "s_waitcnt lgkmcnt(0)\n"\
  "v_mfma_f32_16x16x32_bf16 v[132:135], v[176:179], v[168:171], v[132:135]\n"\
  "s_cbranch_scc0 1b\n"\
  "s_waitcnt lgkmcnt(0)\n"\
  "s_barrier\n"\
  "v_mfma_f32_16x16x32_bf16 v[64:67], v[216:219], v[180:183], v[64:67]\n"\
  "v_mfma_f32_16x16x32_bf16 v[100:103], v[220:223], v[180:183], v[100:103]\n"\
  "v_mfma_f32_16x16x32_bf16 v[68:71], v[216:219], v[184:187], v[68:71]\n"\
  "v_mfma_f32_16x16x32_bf16 v[104:107], v[220:223], v[184:187], v[104:107]\n"\
  "v_mfma_f32_16x16x32_bf16 v[72:75], v[216:219], v[188:191], v[72:75]\n"\
  "v_mfma_f32_16x16x32_bf16 v[108:111], v[220:223], v[188:191], v[108:111]\n"\
  "v_mfma_f32_16x16x32_bf16 v[76:79], v[216:219], v[192:195], v[76:79]\n"\
  "v_mfma_f32_16x16x32_bf16 v[112:115], v[220:223], v[192:195], v[112:115]\n"\
  "v_mfma_f32_16x16x32_bf16 v[80:83], v[216:219], v[196:199], v[80:83]\n"\
  "v_mfma_f32_16x16x32_bf16 v[116:119], v[220:223], v[196:199], v[116:119]\n"\
  "v_mfma_f32_16x16x32_bf16 v[84:87], v[216:219], v[200:203], v[84:87]\n"\
  "v_mfma_f32_16x16x32_bf16 v[120:123], v[220:223], v[200:203], v[120:123]\n"\
  "v_mfma_f32_16x16x32_bf16 v[88:91], v[216:219], v[204:207], v[88:91]\n"\
  "v_mfma_f32_16x16x32_bf16 v[124:127], v[220:223], v[204:207], v[124:127]\n"\
  "v_mfma_f32_16x16x32_bf16 v[92:95], v[216:219], v[208:211], v[92:95]\n"\
  "v_mfma_f32_16x16x32_bf16 v[128:131], v[220:223], v[208:211], v[128:131]\n"\
  "v_mfma_f32_16x16x32_bf16 v[96:99], v[216:219], v[212:215], v[96:99]\n"\
  "v_mfma_f32_16x16x32_bf16 v[132:135], v[220:223], v[212:215], v[132:135]\n"\
  "s_nop 7\n"\
  ""
#define G2_CLOB "v136","v137","v138","v139","v140","v141","v142","v143","v144","v145","v146","v147","v148","v149","v150","v151","v152","v153","v154","v155","v156","v157","v158","v159","v160","v161","v162","v163","v164","v165","v166","v167","v168","v169","v170","v171","v172","v173","v174","v175","v176","v177","v178","v179","v180","v181","v182","v183","v184","v185","v186","v187","v188","v189","v190","v191","v192","v193","v194","v195","v196","v197","v198","v199","v200","v201","v202","v203","v204","v205","v206","v207","v208","v209","v210","v211","v212","v213","v214","v215","v216","v217","v218","v219","v220","v221","v222","v223"
#define G2F_ASM \
  "v_mov_b32 v180, 0\n"\
  "v_mov_b32 v181, 0\n"\
  "v_mov_b32 v182, 0\n"\
  "v_mov_b32 v183, 0\n"\
  "v_mov_b32 v184, 0\n"\
  "v_mov_b32 v185, 0\n"\
  "v_mov_b32 v186, 0\n"\
  "v_mov_b32 v187, 0\n"\
  "v_mov_b32 v188, 0\n"\
  "v_mov_b32 v189, 0\n"\
  "v_mov_b32 v190, 0\n"\
  "v_mov_b32 v191, 0\n"\
  "v_mov_b32 v192, 0\n"\
  "v_mov_b32 v193, 0\n"\
  "v_mov_b32 v194, 0\n"\
  "v_mov_b32 v195, 0\n"\
  "v_mov_b32 v196, 0\n"\
  "v_mov_b32 v197, 0\n"\
  "v_mov_b32 v198, 0\n"\
  "v_mov_b32 v199, 0\n"\
  "v_mov_b32 v200, 0\n"\
  "v_mov_b32 v201, 0\n"\
  "v_mov_b32 v202, 0\n"\
  "v_mov_b32 v203, 0\n"\
  "v_mov_b32 v204, 0\n"\
  "v_mov_b32 v205, 0\n"\
  "v_mov_b32 v206, 0\n"\
  "v_mov_b32 v207, 0\n"\
  "v_mov_b32 v208, 0\n"\
  "v_mov_b32 v209, 0\n"\
  "v_mov_b32 v210, 0\n"\
  "v_mov_b32 v211, 0\n"\
  "v_mov_b32 v212, 0\n"\
  "v_mov_b32 v213, 0\n"\
  "v_mov_b32 v214, 0\n"\
  "v_mov_b32 v215, 0\n"\
  "v_mov_b32 v216, 0\n"\
  "v_mov_b32 v217, 0\n"\
  "v_mov_b32 v218, 0\n"\
  "v_mov_b32 v219, 0\n"\
  "v_mov_b32 v220, 0\n"\
  "v_mov_b32 v221, 0\n"\
  "v_mov_b32 v222, 0\n"\
  "v_mov_b32 v223, 0\n"\
  "1:\n"\
  "s_barrier\n"\
  "v_mfma_f32_16x16x32_bf16 v[64:67], v[216:219], v[180:183], v[64:67]\n"\
  "ds_read_b64_tr_b16 v[172:173], %[t0] offset:0\n"\
  "s_bitcmp1_b32 %[ma], %[cnt]\n"\
  "v_mfma_f32_16x16x32_bf16 v[100:103], v[220:223], v[180:183], v[100:103]\n"\
  "ds_read_b64_tr_b16 v[174:175], %[t0] offset:4096\n"\
  "s_cselect_b32 %[ta], %[wa], %[sa]\n"\
  "v_mfma_f32_16x16x32_bf16 v[68:71], v[216:219], v[184:187], v[68:71]\n"\
  "ds_read_b128 v[136:139], %[ap] offset:0\n"\
  "s_bitcmp1_b32 %[mb], %[cnt]\n"\
  "v_mfma_f32_16x16x32_bf16 v[104:107], v[220:223], v[184:187], v[104:107]\n"\
  "ds_read_b64_tr_b16 v[176:177], %[t1] offset:0\n"\
  "s_cselect_b32 %[tb], %[wb], %[sb]\n"\
  "v_mfma_f32_16x16x32_bf16 v[72:75], v[216:219], v[188:191], v[72:75]\n"\
  "ds_read_b64_tr_b16 v[178:179], %[t1] offset:4096\n"\
  "s_sub_u32 %[cnt], %[cnt], 1\n"\
  "v_mfma_f32_16x16x32_bf16 v[108:111], v[220:223], v[188:191], v[108:111]\n"\
  "ds_read_b128 v[140:143], %[ap] offset:1024\n"\
  "v_mfma_f32_16x16x32_bf16 v[76:79], v[216:219], v[192:195], v[76:79]\n"\
  "ds_read_b128 v[144:147], %[ap] offset:2048\n"\
  "v_mfma_f32_16x16x32_bf16 v[112:115], v[220:223], v[192:195], v[112:115]\n"\
  "ds_read_b128 v[148:151], %[ap] offset:3072\n"\
  "v_mfma_f32_16x16x32_bf16 v[80:83], v[216:219], v[196:199], v[80:83]\n"\
  "ds_read_b128 v[152:155], %[ap] offset:4096\n"\
  "v_mfma_f32_16x16x32_bf16 v[116:119], v[220:223], v[196:199], v[116:119]\n"\
  "ds_read_b128 v[156:159], %[ap] offset:5120\n"\
  "v_mfma_f32_16x16x32_bf16 v[84:87], v[216:219], v[200:203], v[84:87]\n"\
  "ds_read_b128 v[160:163], %[ap] offset:6144\n"\
  "v_mfma_f32_16x16x32_bf16 v[120:123], v[220:223], v[200:203], v[120:123]\n"\
  "ds_read_b128 v[164:167], %[ap] offset:7168\n"\
  "v_mfma_f32_16x16x32_bf16 v[88:91], v[216:219], v[204:207], v[88:91]\n"\
  "ds_read_b128 v[168:171], %[ap] offset:8192\n"\
  "v_mfma_f32_16x16x32_bf16 v[124:127], v[220:223], v[204:207], v[124:127]\n"\
  "v_mfma_f32_16x16x32_bf16 v[92:95], v[216:219], v[208:211], v[92:95]\n"\
  "v_mfma_f32_16x16x32_bf16 v[128:131], v[220:223], v[208:211], v[128:131]\n"\
  "v_mfma_f32_16x16x32_bf16 v[96:99], v[216:219], v[212:215], v[96:99]\n"\
  "v_mfma_f32_16x16x32_bf16 v[132:135], v[220:223], v[212:215], v[132:135]\n"\
  "s_waitcnt lgkmcnt(0)\n"\
  "v_mfma_f32_16x16x32_bf16 v[64:67], v[172:175], v[136:139], v[64:67]\n"\
  "ds_read_b64_tr_b16 v[216:217], %[t0] offset:8192\n"\
  "v_mfma_f32_16x16x32_bf16 v[100:103], v[176:179], v[136:139], v[100:103]\n"\
  "ds_read_b64_tr_b16 v[218:219], %[t0] offset:12288\n"\
  "v_add_u32 %[t0], %[tb], %[t0]\n"\
  "v_mfma_f32_16x16x32_bf16 v[68:71], v[172:175], v[140:143], v[68:71]\n"\
  "ds_read_b128 v[180:183], %[ap] offset:9216\n"\
  "v_mfma_f32_16x16x32_bf16 v[104:107], v[176:179], v[140:143], v[104:107]\n"\
  "ds_read_b64_tr_b16 v[220:221], %[t1] offset:8192\n"\
  "v_mfma_f32_16x16x32_bf16 v[72:75], v[172:175], v[144:147], v[72:75]\n"\
  "ds_read_b64_tr_b16 v[222:223], %[t1] offset:12288\n"\
  "v_add_u32 %[t1], %[tb], %[t1]\n"\
  "v_mfma_f32_16x16x32_bf16 v[108:111], v[176:179], v[144:147], v[108:111]\n"\
  "ds_read_b128 v[184:187], %[ap] offset:10240\n"\
  "v_mfma_f32_16x16x32_bf16 v[76:79], v[172:175], v[148:151], v[76:79]\n"\
  "ds_read_b128 v[188:191], %[ap] offset:11264\n"\
  "v_mfma_f32_16x16x32_bf16 v[112:115], v[176:179], v[148:151], v[112:115]\n"\
  "ds_read_b128 v[192:195], %[ap] offset:12288\n"\
  "v_mfma_f32_16x16x32_bf16 v[80:83], v[172:175], v[152:155], v[80:83]\n"\
  "ds_read_b128 v[196:199], %[ap] offset:13312\n"\
  "v_mfma_f32_16x16x32_bf16 v[116:119], v[176:179], v[152:155], v[116:119]\n"\
  "ds_read_b128 v[200:203], %[ap] offset:14336\n"\
  "v_mfma_f32_16x16x32_bf16 v[84:87], v[172:175], v[156:159], v[84:87]\n"\
  "ds_read_b128 v[204:207], %[ap] offset:15360\n"\
  "v_mfma_f32_16x16x32_bf16 v[120:123], v[176:179], v[156:159], v[120:123]\n"\
  "ds_read_b128 v[208:211], %[ap] offset:16384\n"\
  "v_mfma_f32_16x16x32_bf16 v[88:91], v[172:175], v[160:163], v[88:91]\n"\
  "ds_read_b128 v[212:215], %[ap] offset:17408\n"\
  "v_add_u32 %[ap], %[ta], %[ap]\n"\
  "v_mfma_f32_16x16x32_bf16 v[124:127], v[176:179], v[160:163], v[124:127]\n"\
  "v_mfma_f32_16x16x32_bf16 v[92:95], v[172:175], v[164:167], v[92:95]\n"\
  "v_mfma_f32_16x16x32_bf16 v[128:131], v[176:179], v[164:167], v[128:131]\n"\
  "v_mfma_f32_16x16x32_bf16 v[96:99], v[172:175], v[168:171], v[96:99]\n"\
  "s_waitcnt lgkmcnt(0)\n"\
  "v_mfma_f32_16x16x32_bf16 v[132:135], v[176:179], v[168:171], v[132:135]\n"\
  "s_cbranch_scc0 1b\n"\
  "s_barrier\n"\
  "v_mfma_f32_16x16x32_bf16 v[64:67], v[216:219], v[180:183], v[64:67]\n"\
  "ds_read_b64_tr_b16 v[172:173], %[t0] offset:0\n"\
  "v_mfma_f32_16x16x32_bf16 v[100:103], v[220:223], v[180:183], v[100:103]\n"\
  "ds_read_b64_tr_b16 v[174:175], %[t0] offset:4096\n"\
  "v_mfma_f32_16x16x32_bf16 v[68:71], v[216:219], v[184:187], v[68:71]\n"\
  "ds_read_b128 v[136:139], %[ap] offset:0\n"\
  "v_mfma_f32_16x16x32_bf16 v[104:107], v[220:223], v[184:187], v[104:107]\n"\
  "ds_read_b64_tr_b16 v[176:177], %[t1] offset:0\n"\
  "v_mfma_f32_16x16x32_bf16 v[72:75], v[216:219], v[188:191], v[72:75]\n"\
  "ds_read_b64_tr_b16 v[178:179], %[t1] offset:4096\n"\
  "v_mfma_f32_16x16x32_bf16 v[108:111], v[220:223], v[188:191], v[108:111]\n"\
  "ds_read_b128 v[140:143], %[ap] offset:1024\n"\
  "v_mfma_f32_16x16x32_bf16 v[76:79], v[216:219], v[192:195], v[76:79]\n"\
  "ds_read_b128 v[144:147], %[ap] offset:2048\n"\
  "v_mfma_f32_16x16x32_bf16 v[112:115], v[220:223], v[192:195], v[112:115]\n"\
  "ds_read_b128 v[148:151], %[ap] offset:3072\n"\
  "v_mfma_f32_16x16x32_bf16 v[80:83], v[216:219], v[196:199], v[80:83]\n"\
  "ds_read_b128 v[152:155], %[ap] offset:4096\n"\
  "v_mfma_f32_16x16x32_bf16 v[116:119], v[220:223], v[196:199], v[116:119]\n"\
  "ds_read_b128 v[156:159], %[ap] offset:5120\n"\
  "v_mfma_f32_16x16x32_bf16 v[84:87], v[216:219], v[200:203], v[84:87]\n"\
  "ds_read_b128 v[160:163], %[ap] offset:6144\n"\
  "v_mfma_f32_16x16x32_bf16 v[120:123], v[220:223], v[200:203], v[120:123]\n"\
  "ds_read_b128 v[164:167], %[ap] offset:7168\n"\
  "v_mfma_f32_16x16x32_bf16 v[88:91], v[216:219], v[204:207], v[88:91]\n"\
  "ds_read_b128 v[168:171], %[ap] offset:8192\n"\
  "v_mfma_f32_16x16x32_bf16 v[124:127], v[220:223], v[204:207], v[124:127]\n"\
  "v_mfma_f32_16x16x32_bf16 v[92:95], v[216:219], v[208:211], v[92:95]\n"\
  "v_mfma_f32_16x16x32_bf16 v[128:131], v[220:223], v[208:211], v[128:131]\n"\
  "v_mfma_f32_16x16x32_bf16 v[96:99], v[216:219], v[212:215], v[96:99]\n"\
  "v_mfma_f32_16x16x32_bf16 v[132:135], v[220:223], v[212:215], v[132:135]\n"\
  "s_waitcnt lgkmcnt(8)\n"\
  "v_mfma_f32_16x16x32_bf16 v[64:67], v[172:175], v[136:139], v[64:67]\n"\
  "ds_read_b64_tr_b16 v[216:217], %[t0] offset:8192\n"\
  "ds_read_b64_tr_b16 v[218:219], %[t0] offset:12288\n"\
  "v_mfma_f32_16x16x32_bf16 v[100:103], v[176:179], v[136:139], v[100:103]\n"\
  "ds_read_b128 v[180:183], %[ap] offset:9216\n"\
  "ds_read_b64_tr_b16 v[220:221], %[t1] offset:8192\n"\
  "s_waitcnt lgkmcnt(11)\n"\
  "v_mfma_f32_16x16x32_bf16 v[68:71], v[172:175], v[140:143], v[68:71]\n"\
  "ds_read_b64_tr_b16 v[222:223], %[t1] offset:12288\n"\
  "ds_read_b128 v[184:187], %[ap] offset:10240\n"\
  "v_mfma_f32_16x16x32_bf16 v[104:107], v[176:179], v[140:143], v[104:107]\n"\
  "ds_read_b128 v[188:191], %[ap] offset:11264\n"\
  "ds_read_b128 v[192:195], %[ap] offset:12288\n"\
  "s_waitcnt lgkmcnt(14)\n"\
  "v_mfma_f32_16x16x32_bf16 v[72:75], v[172:175], v[144:147], v[72:75]\n"\
  "ds_read_b128 v[196:199], %[ap] offset:13312\n"\
  "ds_read_b128 v[200:203], %[ap] offset:14336\n"\
  "v_mfma_f32_16x16x32_bf16 v[108:111], v[176:179], v[144:147], v[108:111]\n"\
  "ds_read_b128 v[204:207], %[ap] offset:15360\n"\
  "ds_read_b128 v[208:211], %[ap] offset:16384\n"\
  "s_waitcnt lgkmcnt(15)\n"\
  "v_mfma_f32_16x16x32_bf16 v[76:79], v[172:175], v[148:151], v[76:79]\n"\
  "ds_read_b128 v[212:215], %[ap] offset:17408\n"\
  "v_mfma_f32_16x16x32_bf16 v[112:115], v[176:179], v[148:151], v[112:115]\n"\
  "s_waitcnt lgkmcnt(8)\n"\
  "v_mfma_f32_16x16x32_bf16 v[64:67], v[216:219], v[180:183], v[64:67]\n"\
  "v_mfma_f32_16x16x32_bf16 v[100:103], v[220:223], v[180:183], v[100:103]\n"\
  "v_mfma_f32_16x16x32_bf16 v[80:83], v[172:175], v[152:155], v[80:83]\n"\
  "v_mfma_f32_16x16x32_bf16 v[116:119], v[176:179], v[152:155], v[116:119]\n"\
  "s_waitcnt lgkmcnt(7)\n"\
  "v_mfma_f32_16x16x32_bf16 v[68:71], v[216:219], v[184:187], v[68:71]\n"\
  "v_mfma_f32_16x16x32_bf16 v[104:107], v[220:223], v[184:187], v[104:107]\n"\
  "v_cvt_pk_bf16_f32 v136, v64, v65\n"\
  "v_cvt_pk_bf16_f32 v137, v66, v67\n"\
  "v_mfma_f32_16x16x32_bf16 v[84:87], v[172:175], v[156:159], v[84:87]\n"\
  "v_cvt_pk_bf16_f32 v138, v100, v101\n"\
  "v_cvt_pk_bf16_f32 v139, v102, v103\n"\
  "global_store_dwordx4 %[vo], v[136:139], %[sp] offset:-4096 sc1\n"\
  "v_mfma_f32_16x16x32_bf16 v[120:123], v[176:179], v[156:159], v[120:123]\n"\
  "s_waitcnt lgkmcnt(6)\n"\
  "v_mfma_f32_16x16x32_bf16 v[72:75], v[216:219], v[188:191], v[72:75]\n"\
  "v_mfma_f32_16x16x32_bf16 v[108:111], v[220:223], v[188:191], v[108:111]\n"\
  "v_cvt_pk_bf16_f32 v140, v68, v69\n"\
  "v_cvt_pk_bf16_f32 v141, v70, v71\n"\
  "v_mfma_f32_16x16x32_bf16 v[88:91], v[172:175], v[160:163], v[88:91]\n"\
  "v_cvt_pk_bf16_f32 v142, v104, v105\n"\
  "v_cvt_pk_bf16_f32 v143, v106, v107\n"\
  "global_store_dwordx4 %[vo], v[140:143], %[sp] offset:-3072 sc1\n"\
  "v_mfma_f32_16x16x32_bf16 v[124:127], v[176:179], v[160:163], v[124:127]\n"\
  "s_waitcnt lgkmcnt(5)\n"\
  "v_mfma_f32_16x16x32_bf16 v[76:79], v[216:219], v[192:195], v[76:79]\n"\
  "v_mfma_f32_16x16x32_bf16 v[112:115], v[220:223], v[192:195], v[112:115]\n"\
  "v_cvt_pk_bf16_f32 v144, v72, v73\n"\
  "v_cvt_pk_bf16_f32 v145, v74, v75\n"\
  "v_mfma_f32_16x16x32_bf16 v[92:95], v[172:175], v[164:167], v[92:95]\n"\
  "v_cvt_pk_bf16_f32 v146, v108, v109\n"\
  "v_cvt_pk_bf16_f32 v147, v110, v111\n"\
  "global_store_dwordx4 %[vo], v[144:147], %[sp] offset:-2048 sc1\n"\
  "v_mfma_f32_16x16x32_bf16 v[128:131], v[176:179], v[164:167], v[128:131]\n"\
  "s_waitcnt lgkmcnt(4)\n"\
  "v_mfma_f32_16x16x32_bf16 v[80:83], v[216:219], v[196:199], v[80:83]\n"\
  "v_mfma_f32_16x16x32_bf16 v[116:119], v[220:223], v[196:199], v[116:119]\n"\
  "v_cvt_pk_bf16_f32 v148, v76, v77\n"\
  "v_cvt_pk_bf16_f32 v149, v78, v79\n"\
  "v_mfma_f32_16x16x32_bf16 v[96:99], v[172:175], v[168:171], v[96:99]\n"\
  "v_cvt_pk_bf16_f32 v150, v112, v113\n"\
  "v_cvt_pk_bf16_f32 v151, v114, v115\n"\
  "global_store_dwordx4 %[vo], v[148:151], %[sp] offset:-1024 sc1\n"\
  "v_mfma_f32_16x16x32_bf16 v[132:135], v[176:179], v[168:171], v[132:135]\n"\
  "s_waitcnt lgkmcnt(3)\n"\
  "v_mfma_f32_16x16x32_bf16 v[84:87], v[216:219], v[200:203], v[84:87]\n"\
  "v_mfma_f32_16x16x32_bf16 v[120:123], v[220:223], v[200:203], v[120:123]\n"\
  "v_cvt_pk_bf16_f32 v152, v80, v81\n"\
  "v_cvt_pk_bf16_f32 v153, v82, v83\n"\
  "s_waitcnt lgkmcnt(2)\n"\
  "v_mfma_f32_16x16x32_bf16 v[88:91], v[216:219], v[204:207], v[88:91]\n"\
  "v_cvt_pk_bf16_f32 v154, v116, v117\n"\
  "v_cvt_pk_bf16_f32 v155, v118, v119\n"\
  "global_store_dwordx4 %[vo], v[152:155], %[sp] offset:0 sc1\n"\
  "v_mfma_f32_16x16x32_bf16 v[124:127], v[220:223], v[204:207], v[124:127]\n"\
  "s_waitcnt lgkmcnt(1)\n"\
  "v_mfma_f32_16x16x32_bf16 v[92:95], v[216:219], v[208:211], v[92:95]\n"\
  "v_mfma_f32_16x16x32_bf16 v[128:131], v[220:223], v[208:211], v[128:131]\n"\
  "v_cvt_pk_bf16_f32 v156, v84, v85\n"\
  "v_cvt_pk_bf16_f32 v157, v86, v87\n"\
  "s_waitcnt lgkmcnt(0)\n"\
  "v_mfma_f32_16x16x32_bf16 v[96:99], v[216:219], v[212:215], v[96:99]\n"\
  "v_cvt_pk_bf16_f32 v158, v120, v121\n"\
  "v_cvt_pk_bf16_f32 v159, v122, v123\n"\
  "global_store_dwordx4 %[vo], v[156:159], %[sp] offset:1024 sc1\n"\
  "v_mfma_f32_16x16x32_bf16 v[132:135], v[220:223], v[212:215], v[132:135]\n"\
  "v_cvt_pk_bf16_f32 v160, v88, v89\n"\
  "v_cvt_pk_bf16_f32 v161, v90, v91\n"\
  "v_cvt_pk_bf16_f32 v162, v124, v125\n"\
  "v_cvt_pk_bf16_f32 v163, v126, v127\n"\
  "global_store_dwordx4 %[vo], v[160:163], %[sp] offset:2048 sc1\n"\
  "s_nop 7\n"\
  "v_cvt_pk_bf16_f32 v164, v92, v93\n"\
  "v_cvt_pk_bf16_f32 v165, v94, v95\n"\
  "v_cvt_pk_bf16_f32 v166, v128, v129\n"\
  "v_cvt_pk_bf16_f32 v167, v130, v131\n"\
  "global_store_dwordx4 %[vo], v[164:167], %[sp] offset:3072 sc1\n"\
  "v_cvt_pk_bf16_f32 v168, v96, v97\n"\
  "v_cvt_pk_bf16_f32 v169, v98, v99\n"\
  "v_cvt_pk_bf16_f32 v170, v132, v133\n"\
  "v_cvt_pk_bf16_f32 v171, v134, v135\n"\
  "global_store_dwordx4 %[vo], v[168:171], %[sq] offset:0 sc1\n"\
  "s_barrier\n"\
  ""
#define G2F_CLOB "v136","v137","v138","v139","v140","v141","v142","v143","v144","v145","v146","v147","v148","v149","v150","v151","v152","v153","v154","v155","v156","v157","v158","v159","v160","v161","v162","v163","v164","v165","v166","v167","v168","v169","v170","v171","v172","v173","v174","v175","v176","v177","v178","v179","v180","v181","v182","v183","v184","v185","v186","v187","v188","v189","v190","v191","v192","v193","v194","v195","v196","v197","v198","v199","v200","v201","v202","v203","v204","v205","v206","v207","v208","v209","v210","v211","v212","v213","v214","v215","v216","v217","v218","v219","v220","v221","v222","v223"

constexpr int Q_STAGE = 32768, Q_AUX = 32768, Q_JUNK = 131072;
constexpr int A_KS = 1088, A_VS = 1040, A_QS = 576;
constexpr int A_K = 0, A_V = 20 * A_KS, A_Q = A_V + 19 * A_VS, A_END = A_Q + 18 * A_QS, A_JUNK = 52224;
constexpr int P_WO = 53248, P_O = 53248, P_AUX = 131072;
constexpr int G1_B = 0, G1_A = 114688;
constexpr int G2_B = 0, G2_A = 81920;
constexpr int F_H = 49152;
constexpr int LDS_BYTES = 163840;
static_assert(A_END <= A_JUNK, "lds map");

__global__ __launch_bounds__(512, 2) void mega(const u16* __restrict__ x, const u16* __restrict__ wqkv, const u16* __restrict__ wo,
                                               const u16* __restrict__ w1, const u16* __restrict__ b1, const u16* __restrict__ w2,
                                               const u16* __restrict__ b2, u16* __restrict__ out, char* __restrict__ ws) {
  extern __shared__ __attribute__((aligned(16))) char smem[];
  asm volatile("mega_code_begin:");
  const int wg = blockIdx.x, tid = threadIdx.x, l = tid & 63;
  const int wid = __builtin_amdgcn_readfirstlane(tid >> 6), lw = wid & 3;
  const int xcd = wg & 7, idx = wg >> 3;
  const unsigned sb = (unsigned)(size_t)smem;
  const int i16 = l & 15, g = l >> 4, q = (l & 15) >> 2, p = l & 3, mh = lw >> 1, nh = lw & 1;
  u32x2 tag;
  auto get_tag = [&]() {
    u64 e;
    asm volatile("s_load_dwordx2 %0, %1, 0x0\n\ts_waitcnt lgkmcnt(0)" : "=&s"(e) : "s"(ws) : "memory");
    e = e * 6364136223846793005ull + 1442695040888963407ull;
    tag = (u32x2){(unsigned)e, (unsigned)(e >> 32)};
  };
  auto pf_code = [&](int i, unsigned junk) {
    const char* pc; int fwd; unsigned len;
    asm volatile("s_getpc_b64 %0\n1:\n\ts_mov_b32 %1, mega_code_end-1b\n\ts_mov_b32 %2, mega_code_end-mega_code_begin" : "=s"(pc), "=s"(fwd), "=s"(len));
    touch(pc + fwd - min((unsigned)(i * 64 + l) * 128u + 4u, len), junk);
  };
  const int rot1 = ROT ? (2 * (xcd & 3) + (xcd >> 2)) * 4 : 0, rot2 = ROT ? (xcd & 1) * 10 : 0;
  auto ad_w1 = [&](int i) {
    const unsigned n = i * 64 + l, row = n / 10;
    return (const char*)w1 + (size_t)((row + 64 * rot1) & 2047) * 9216 + ((27 * xcd) / 6) * 256 + (n - 10 * row) * 128;
  };
  auto ad_w2 = [&](int i) {
    i += 16 * rot2; i = i >= 288 ? i - 288 : i;
    return (const char*)w2 + (size_t)(1152 * (xcd >> 1) + 4 * i + g) * 4096 + (xcd & 1) * 2048 + i16 * 128;
  };
  auto pf_w1 = [&](int i, unsigned junk) { touch(ad_w1(i), junk); };
  auto pf_w2 = [&](int i, unsigned junk) { touch(ad_w2(i), junk); };

  auto row_stats = [&]() {
    typedef __attribute__((ext_vector_type(3))) unsigned u32x3;
    const int row = 512 * (xcd & 3) + 8 * ((xcd >> 2) * 32 + idx) + 2 * lw + (l >> 5);
    const char* px = (const char*)x + (size_t)row * 1152 + (l & 31) * 12;
    u32x3 v0, v1, v2;
    asm volatile("global_load_dwordx3 %0, %3, off\n\tglobal_load_dwordx3 %1, %3, off offset:384\n\tglobal_load_dwordx3 %2, %3, off offset:768"
                 : "=&v"(v0), "=&v"(v1), "=&v"(v2) : "v"(px) : "memory");
    get_tag();
    asm volatile("s_waitcnt vmcnt(0)" : "+v"(v0), "+v"(v1), "+v"(v2)::"memory");
    float a = 0, b = 0;
#pragma unroll
    for (int e = 0; e < 3; ++e) {
      a = dot2(v0[e], 0x3f803f80u, a); b = dot2(v0[e], v0[e], b);
      a = dot2(v1[e], 0x3f803f80u, a); b = dot2(v1[e], v1[e], b);
      a = dot2(v2[e], 0x3f803f80u, a); b = dot2(v2[e], v2[e], b);
    }
    a = row16_sum(a); b = row16_sum(b);
    a = dpp_add<0x142>(a); b = dpp_add<0x142>(b);
    const float mu = a * (1.f / 576.f);
    const float r = __builtin_amdgcn_rsqf(fmaxf(b * (1.f / 576.f) - mu * mu, 0.f) + 1e-5f);
    if ((l & 31) == 16) st16_wt(ws + WS_ROWST + row * 16, (u32x4){__float_as_uint(r), __float_as_uint(-mu * r), tag[0], tag[1]});
  };
  int q_flag = 0;
  if (wg >= 224) {
    const int si = idx - 28, k = (xcd & 3) * 4 + si;
    const int slab = (xcd >> 2) * 14 + k;
    const bool docol = k < 14 && slab < 27;
    const char* pw = (const char*)wqkv + (size_t)(8 * wid + (l >> 3)) * 3456 + (docol ? slab : 0) * 128 + (l & 7) * 16;
    u32x4 vw[9];
#pragma unroll
    for (int c = 0; c < 9; ++c) vw[c] = *(const u32x4*)(pw + c * 64 * 3456);
    if (PFI && wid == 7) { pf_code(si, sb + Q_JUNK); if (si == 0) pf_code(4, sb + Q_JUNK); }
    if (wid < 4) row_stats(); else get_tag();
    unsigned klo = 0x00003f80u, khi = 0x3f800000u;
    asm volatile("" : "+s"(klo), "+s"(khi));
    float cs[8] = {0, 0, 0, 0, 0, 0, 0, 0};
#pragma unroll
    for (int c = 0; c < 9; ++c)
#pragma unroll
      for (int e = 0; e < 4; ++e) {
        cs[2 * e] = dot2(vw[c][e], klo, cs[2 * e]);
        cs[2 * e + 1] = dot2(vw[c][e], khi, cs[2 * e + 1]);
      }
#pragma unroll
    for (int e = 0; e < 8; ++e) cs[e] = dpp_add<0x128>(cs[e]);
    if ((l & 8) == 0) {
      const unsigned ca = sb + (wid * 4 + g) * 256 + (l & 7) * 32;
      *(LDS3 f32x4*)(ca) = (f32x4){cs[0], cs[1], cs[2], cs[3]};
      *(LDS3 f32x4*)(ca + 16) = (f32x4){cs[4], cs[5], cs[6], cs[7]};
    }
    wait_lgkm0(); bar();
    if (wid == 0) {
      f32x2 t = {0, 0};
#pragma unroll
      for (int j = 0; j < 16; ++j) t += *(const LDS3 f32x2*)(sb + (16 * (l >> 5) + j) * 256 + (l & 31) * 8);
      t[0] += __shfl_xor(t[0], 32); t[1] += __shfl_xor(t[1], 32);
      if (l < 32 && docol) st16_wt(ws + WS_COLSM + (slab * 32 + l) * 16, (u32x4){__float_as_uint(t[0]), __float_as_uint(t[1]), tag[0], tag[1]});
      wait_lgkm0();
    }
    bar();
    if (PFWO) {
#pragma unroll 1
      for (int i = si * 8 + wid; i < 81 + PFA; i += 32) {
        if (i < 81) touch((const char*)wo + (size_t)(i * 64 + l) * 128, sb + Q_JUNK + wid * 256);
        else pf_w1(i - 81, sb + Q_JUNK + wid * 256);
      }
    }
  } else {
    const int tb = (xcd & 3) * 4 + (idx & 3), ct = (xcd >> 2) * 7 + (idx >> 2);
    if (wid >= 4) {
      const unsigned vx = (8 * lw + (l >> 3)) * 1152 + (((l & 7) ^ (((l >> 4) & 1) | (lw << 1))) * 16);
      int cw = (l & 15) ^ ((g | ((lw >> 1) << 2)) << 1);
      if (ct == 13) cw &= 7;
      const unsigned vw = (4 * lw + g) * 3456 + cw * 16;
      const char* px = (const char*)x + (size_t)tb * 128 * 1152;
      const char* pw = (const char*)wqkv + ct * 256;
      auto issue = [&](int s) {
        const unsigned slot = sb + (s & 3) * Q_STAGE + lw * 1024;
#pragma unroll
        for (int i = 0; i < 4; ++i) glds<0>(px + i * 32 * 1152, vx, slot + i * 4096);
#pragma unroll
        for (int i = 0; i < 4; ++i) glds<0>(pw + i * 16 * 3456, vw, slot + 16384 + i * 4096);
        px += 128; pw += 64 * 3456;
      };
      issue(0);
      if (PF0) {
        const int xr = min(19 * (idx >> 2) + min(l - 32, 18), 127);
        const char* a = l < 32 ? (const char*)wqkv + (size_t)(16 * (idx & 3) + (l >> 1)) * 3456 + ct * 256 + (ct == 13 ? 0 : (l & 1) * 128)
                               : (const char*)x + (size_t)(128 * tb + xr) * 1152;
        const unsigned stp = l < 32 ? 64 * 3456 : 128;
        touch(a + (size_t)(1 + lw) * stp, sb + Q_JUNK + lw * 256);
        if (PF0 == 1) { touch(a + (size_t)(5 + lw) * stp, sb + Q_JUNK + lw * 256); get_tag(); wait_vm<2>(); bar(); issue(1); }
        else { get_tag(); wait_vm<1>(); bar(); issue(1); touch(a + (size_t)(5 + lw) * stp, sb + Q_JUNK + lw * 256); }
      } else { issue(1); get_tag(); wait_vm<8>(); bar(); }
      issue(2); issue(3);
#pragma unroll 1
      for (int s = 1; s < 6; ++s) { wait_vm<16>(); bar(); issue(s + 3); }
      wait_vm<16>(); bar();
      wait_vm<8>(); bar();
      const char* gp = lw < 2 ? ws + WS_ROWST + (128 * tb + 64 * lw + l) * 16 : ws + WS_COLSM + (64 * ct + (ct == 13 ? (l & 31) : l)) * 16;
      u32x4 gv = ld16_dev(gp);
      bar();
    #pragma unroll 1
  for (int spin = 0; spin < SPIN_MAX && !__all(gv[2] == tag[0] && gv[3] == tag[1]); ++spin) gv = ld16_dev(gp);
      if (lw < 2) { *(LDS3 unsigned*)(sb + Q_AUX + (64 * lw + l) * 4) = gv[0]; *(LDS3 unsigned*)(sb + Q_AUX + 512 + (64 * lw + l) * 4) = gv[1]; }
      if (lw == 2) *(LDS3 u32x2*)(sb + Q_AUX + 1024 + l * 8) = (u32x2){gv[0], gv[1]};
      wait_lgkm0(); bar();
    } else {
      row_stats();
      unsigned ap = sb + (64 * mh + 8 * (i16 >> 2) + (i16 & 3)) * 128 + ((g ^ (i16 >> 1)) * 16), aq = ap ^ 64;
      unsigned aw[4];
#pragma unroll
      for (int c = 0; c < 4; ++c) aw[c] = sb + 16384 + (8 * g + q) * 256 + (((4 * nh + c) ^ (q | ((g & 1) << 2))) * 32) + 8 * p;
      f32x16 acc[4] = {};
      unsigned cnt = 8, ta;
      asm volatile(GQ_ASM : "+{v[64:79]}"(acc[0]), "+{v[80:95]}"(acc[1]), "+{v[96:111]}"(acc[2]), "+{v[112:127]}"(acc[3]), [ap] "+v"(ap), [aq] "+v"(aq),
                   [t0] "+v"(aw[0]), [t1] "+v"(aw[1]), [t2] "+v"(aw[2]), [t3] "+v"(aw[3]), [cnt] "+s"(cnt), [ta] "=&s"(ta)
                   : [ma] "s"(wrap_mask(9, 4)), [sa] "s"(Q_STAGE), [wa] "s"(-3 * Q_STAGE) : "memory", "scc", GQ_CLOB);
      if (!(ct == 13 && nh == 1)) {
        const float sc = 2 * ct + nh < 9 ? 0.23570226039551584f * 1.4426950408889634f : 1.f;
        const char* qb = ws + WS_QKVT + (size_t)(128 * ct + 64 * nh) * QKV_RS + (128 * tb + 64 * mh) * 2;
        const unsigned vo = (l >> 3) * QKV_RS + (l & 7) * 16;
        const unsigned tw = sb + 65536 + lw * 8192 + i16 * 128 + ((g ^ (i16 & 7)) * 16), tr = sb + 65536 + lw * 8192 + (l >> 3) * 128 + (((l & 7) ^ (l >> 3)) * 16);
        float csum[4];
#pragma unroll
        for (int c = 0; c < 4; ++c) csum[c] = *(const LDS3 float*)(sb + Q_AUX + 1024 + (64 * nh + 16 * c + i16) * 4);
        f32x8 ra[2], rb[2];
#pragma unroll
        for (int a = 0; a < 2; ++a) {
          ra[a] = *(const LDS3 f32x8*)(sb + Q_AUX + (64 * mh + 32 * a + 8 * g) * 4) * sc;
          rb[a] = *(const LDS3 f32x8*)(sb + Q_AUX + 512 + (64 * mh + 32 * a + 8 * g) * 4) * sc;
        }
#pragma unroll
        for (int c = 0; c < 4; ++c) {
          u32x4 o[2];
#pragma unroll
          for (int a = 0; a < 2; ++a)
#pragma unroll
            for (int e = 0; e < 4; ++e) {
              const f32x2 t = (f32x2){rb[a][2 * e], rb[a][2 * e + 1]} * csum[c];
              const f32x2 v = __builtin_elementwise_fma((f32x2){acc[c][8 * a + 2 * e], acc[c][8 * a + 2 * e + 1]}, (f32x2){ra[a][2 * e], ra[a][2 * e + 1]}, t);
              o[a][e] = pk2(v[0], v[1]);
            }
          *(LDS3 u32x4*)(tw + c * 2048) = o[0];
          *(LDS3 u32x4*)((tw ^ 64) + c * 2048) = o[1];
          const u32x4 n0 = *(const LDS3 u32x4*)(tr + c * 2048), n1 = *(const LDS3 u32x4*)(tr + c * 2048 + 1024);
          st16_s<0>(qb + c * 16 * QKV_RS, vo, n0); st16_s<0>(qb + (c * 16 + 8) * QKV_RS, vo, n1);
        }
      }
      q_flag = tb * 14 + ct;
    }
  }

  const int p_tt = (2 * (xcd & 3) + (xcd >> 2)) * 4 + (idx & 3), p_ct = idx >> 2;
  const int m_tile = xcd * 27 + idx, m_ft = m_tile / 6, m_dt = m_tile - 6 * m_ft;
  u32x2 bb0 = {0, 0}, bb1 = {0, 0};
  const int kq = xcd >> 1, st = (xcd & 1) * 8 + (idx >> 2), dt = idx & 3, tile = st * 4 + dt;
  char* part = ws + (PQ ? WS_QKVT : WS_PART) + (size_t)tile * 12 * 9216;
  bf16x8 xr[2][2] = {};
  {
    const int head = xcd * 4 + (idx & 3), tb = idx >> 2;
    unsigned vm[3], vt[3];
#pragma unroll
    for (int n = 0; n < 3; ++n) {
      const int c = 64 * (3 * lw + n) + l, R = c / 12, pos = c - 12 * R, rho = R & 31, ks = R >> 5;
      const int ch = pos ^ (((rho >> 2) & 1) << 1);
      vm[n] = (36 * ks + 18 * ((rho >> 3) & 1) + 8 * (rho >> 4) + (rho & 7)) * 1152 + ch * 16;
      const int hd = 16 * ks + 4 * ((rho >> 2) & 3) + 2 * (rho >> 4) + ((rho >> 1) & 1);
      vt[n] = wid >= 4 ? (18 * hd + 16 + (rho & 1)) * 1152 + ch * 16 : vm[n] + 3 * 72 * 1152;
    }
    const int s0 = wid >= 4 ? 5 : 0;
    const char* bw0 = (const char*)wo + min(p_ct, 5) * 192;
    const char* bw = bw0 + s0 * 72 * 1152;
    const unsigned lw0 = sb + P_WO + s0 * 12288 + 3 * lw * 1024;
    int m0_ = (wg - NOWO) >> 31, m1_ = m0_ & ((wid - 4) >> 31);
    asm volatile("" : "+s"(m0_), "+s"(m1_));
    const u64 on0 = (u64)(long long)m0_, on1 = (u64)(long long)m1_;
    auto wo_piece = [&](int j) {
      const int s = j / 3, n = j % 3;
      if (s == 3) glds_m(bw0, vt[n], lw0 + s * 12288 + n * 1024, on0);
      else if (s < 5) glds_m(bw + s * 72 * 1152, vm[n], lw0 + s * 12288 + n * 1024, s < 3 ? on0 : on1);
    };
    const int h = l >> 5, blk = g & 1, i32 = l & 31, pp = ((p & 1) << 1) | (p >> 1), dd = i32 - 8 * h;
    unsigned aq = sb + A_Q + (8 * h + q) * A_QS + (32 * wid + 16 * blk + 4 * p) * 2;
    unsigned aq1 = sb + A_Q + (16 + (q & 1)) * A_QS + (32 * wid + 16 * blk + 4 * p) * 2;
    unsigned ak0 = sb + A_K + (8 * h + q) * A_KS + (32 * wid + 16 * blk + 4 * pp) * 2;
    unsigned ak1 = sb + A_K + (16 + q) * A_KS + (32 * wid + 16 * blk + 4 * pp) * 2;
    unsigned av = sb + A_V + (i32 < 19 ? i32 : (i32 >= 20 && i32 < 28) ? 0 : 4) * A_VS + (32 * wid + 8 * h) * 2;
    unsigned mk[8];
#pragma unroll
    for (int r = 0; r < 8; ++r) { const int j = 16 * (r >> 2) + 2 * (r & 3); mk[r] = (j + 1 <= dd) ? 0xffffffffu : ((j <= dd) ? 0x0000ffffu : 0u); }
    asm volatile("" : "+v"(aq), "+v"(aq1), "+v"(ak0), "+v"(ak1), "+v"(av), "+v"(mk[0]), "+v"(mk[1]), "+v"(mk[2]), "+v"(mk[3]), "+v"(mk[4]), "+v"(mk[5]), "+v"(mk[6]), "+v"(mk[7]),
                 "+v"(vm[0]), "+v"(vm[1]), "+v"(vm[2]), "+v"(vt[0]), "+v"(vt[1]), "+v"(vt[2]));
    const int sec = min(l >> 3, 2), t = sec == 0 ? ((l & 3) | 2) : (l & 3);
    const char* fq = ws + WS_F_QKV + (max(2 * tb - 2 + t, 0) * 14 + ((576 * sec + 18 * head + 17 * ((l >> 2) & 1)) >> 7)) * FPI;
    asm volatile("" : "+v"(fq));
    if (wg < 224) {
      if (wid < 4) wait_vm<0>();
      wait_lgkm0(); bar();
      if (tid == 0) st8_wt(ws + WS_F_QKV + q_flag * FPI, tag);
    }
    if (tid < 256) {
      *(LDS3 unsigned*)(sb + A_K + 18 * A_KS + tid * 4) = (tb == 0 && tid < 128) ? 0xf149f149u : 0u;
      *(LDS3 unsigned*)(sb + A_K + 19 * A_KS + tid * 4) = 0u;
    } else {
      *(LDS3 unsigned*)(sb + A_V + 18 * A_VS + (tid - 256) * 4) = 0x3f803f80u;
    }
    if (wid == 4 && l < 24) pollp<PP & 1, DL1>(fq, tag);
    wait_lgkm0();
    bar();
    const char* qkvT = ws + WS_QKVT;
    {
      int key0 = 256 * tb - 256;
      unsigned vk = l * 16;
      if (tb == 0) { key0 = 0; vk = (l & 31) * 16; }
      const char* bk = qkvT + (size_t)(576 + 18 * head) * QKV_RS + key0 * 2;
      const char* bv = bk + (size_t)576 * QKV_RS;
      const char* bq = qkvT + (size_t)(18 * head) * QKV_RS + tb * 512;
      for (int r = wid; r < 18; r += 8) {
        glds<0, 2>(bk + r * QKV_RS, vk, sb + A_K + r * A_KS);
        if (l < 32) glds<0, 2>(bq + r * QKV_RS, l * 16, sb + A_Q + r * A_QS);
      }
      for (int r = 7 - wid; r < 18; r += 8) glds<0, 2>(bv + r * QKV_RS, vk, sb + A_V + r * A_VS);
    }
    if (VSPLIT) { if (wid < 6) wait_vm<2>(); else wait_vm<3>(); } else wait_vm<0>();
    wait_lgkm0(); bar();
    bf16x8 q0; s16x4 q1;
    {
      q0 = cat(lds_tr(aq), lds_tr(aq + 4 * A_QS));
      u32x2 t = __builtin_bit_cast(u32x2, lds_tr(aq1));
      t[0] = h ? 0u : t[0];
      t[1] = h ? 0u : 0x00003f80u;
      q1 = __builtin_bit_cast(s16x4, t);
    }
    auto qk = [&](int t) {
      bf16x8 k0 = cat(lds_tr(ak0 + 64 * t), lds_tr(ak0 + 64 * t + 4 * A_KS));
      s16x4 k1 = __builtin_bit_cast(s16x4, lds_tr(ak1 + 64 * t));
      f32x16 z = {};
      z = __builtin_amdgcn_mfma_f32_32x32x16_bf16(k0, q0, z, 0, 0, 0);
      return __builtin_amdgcn_mfma_f32_32x32x8bf16_1k(k1, q1, z, 0, 0, 0);
    };
    f32x16 o = {};
    auto pv = [&](int t, u32x4 p0, u32x4 p1) {
      o = __builtin_amdgcn_mfma_f32_32x32x16_bf16(lds_b128(av + 64 * t), __builtin_bit_cast(bf16x8, p0), o, 0, 0, 0);
      o = __builtin_amdgcn_mfma_f32_32x32x16_bf16(lds_b128(av + 64 * t + 32), __builtin_bit_cast(bf16x8, p1), o, 0, 0, 0);
    };
    {
#define SB __builtin_amdgcn_sched_barrier(0)
      auto rk0 = [&](int t) { return cat(lds_tr(ak0 + 64 * t), lds_tr(ak0 + 64 * t + 4 * A_KS)); };
      auto rk1 = [&](int t) { return __builtin_bit_cast(s16x4, lds_tr(ak1 + 64 * t)); };
      const f32x16 zero = {};
      bf16x8 k0 = rk0(2); s16x4 k1 = rk1(2);
      f32x16 far = qk(0), cur = qk(1);
      SB;
      if (VSPLIT) { wait_vm<0>(); bar(); }
    if (PFB && wg >= 192 && wid < 4) pf_w1(PFA + (idx - 24) * 4 + wid, sb + A_JUNK);
    if (wg < 192 && wid < 4) {
#pragma unroll
      for (int m = 0; m < 2; ++m)
#pragma unroll
        for (int w = 0; w < 2; ++w)
          xr[m][w] = *(const bf16x8*)(x + (size_t)(64 * p_tt + 32 * mh + 16 * m + i16) * 576 + 96 * p_ct + 48 * nh + 16 * w + 8 * g);
    }
      bf16x8 va = lds_b128(av + 64), vb = va;
      u32x4 pb = {};
      auto ex = [&](int r) { return pk2(__builtin_amdgcn_exp2f(cur[2 * r]), __builtin_amdgcn_exp2f(cur[2 * r + 1])); };
#pragma unroll
      for (int t = 1; t < 8; ++t) {
        u32x4 pa;
        SB;
        f32x16 nxt = __builtin_amdgcn_mfma_f32_32x32x16_bf16(k0, q0, zero, 0, 0, 0);
        SB;
        if (t < 7) k0 = rk0(t + 2);
        pa[0] = ex(0); pa[1] = ex(1);
        SB;
        if (t > 1) o = __builtin_amdgcn_mfma_f32_32x32x16_bf16(vb, __builtin_bit_cast(bf16x8, pb), o, 0, 0, 0);
        SB;
        vb = lds_b128(av + 64 * t + 32);
        wo_piece(2 * t - 2);
        pa[2] = ex(2); pa[3] = ex(3);
        SB;
        nxt = __builtin_amdgcn_mfma_f32_32x32x8bf16_1k(k1, q1, nxt, 0, 0, 0);
        SB;
        if (t < 7) k1 = rk1(t + 2);
        pb[0] = ex(4); pb[1] = ex(5);
        SB;
        o = __builtin_amdgcn_mfma_f32_32x32x16_bf16(va, __builtin_bit_cast(bf16x8, pa), o, 0, 0, 0);
        SB;
        va = lds_b128(av + 64 * (t + 1));
        wo_piece(2 * t - 1);
        if (PFB && ATN && t == PFT && wg >= 192 && wid >= 8 - ATN) pf_w1(PFA + 32 + (idx - 24) * ATN + 7 - wid, sb + A_JUNK);
        pb[2] = ex(6); pb[3] = ex(7);
        cur = nxt;
      }
      SB;
      o = __builtin_amdgcn_mfma_f32_32x32x16_bf16(vb, __builtin_bit_cast(bf16x8, pb), o, 0, 0, 0);
      vb = lds_b128(av + 64 * 8 + 32);
      const bf16x8 vc = lds_b128(av), vd = lds_b128(av + 32);
      wo_piece(14);
#pragma unroll
      for (int r = 0; r < 16; ++r) cur[r] = (16 * (r >> 3) + (r & 7) <= dd) ? cur[r] : far[r];
      u32x4 pd, pf;
#pragma unroll
      for (int r = 0; r < 4; ++r) { const unsigned e = ex(r); pd[r] = e & mk[r]; pf[r] = e ^ pd[r]; }
      SB;
      o = __builtin_amdgcn_mfma_f32_32x32x16_bf16(va, __builtin_bit_cast(bf16x8, pd), o, 0, 0, 0);
      o = __builtin_amdgcn_mfma_f32_32x32x16_bf16(vc, __builtin_bit_cast(bf16x8, pf), o, 0, 0, 0);
      SB;
#pragma unroll
      for (int r = 0; r < 4; ++r) { const unsigned e = ex(4 + r); pd[r] = e & mk[4 + r]; pf[r] = e ^ pd[r]; }
      o = __builtin_amdgcn_mfma_f32_32x32x16_bf16(vb, __builtin_bit_cast(bf16x8, pd), o, 0, 0, 0);
      o = __builtin_amdgcn_mfma_f32_32x32x16_bf16(vd, __builtin_bit_cast(bf16x8, pf), o, 0, 0, 0);
    }
    float lsum = __shfl(o[10], i32);
    if (!__all(lsum >= 0x1p-80f && lsum <= 0x1p80f)) {
      const int lo = dd + 1;
      float m = -3e38f;
#pragma unroll 1
      for (int t = 0; t < 9; ++t) {
        const f32x16 z = qk(t);
#pragma unroll
        for (int r = 0; r < 16; ++r) { const unsigned j = 32 * t + 16 * (r >> 3) + (r & 7) - lo; m = __builtin_fmaxf(m, j < 256u ? z[r] : m); }
      }
      m = __builtin_fmaxf(m, __shfl_xor(m, 32));
      o = (f32x16){};
#pragma unroll 1
      for (int t = 0; t < 9; ++t) {
        const f32x16 z = qk(t);
        float e[16];
#pragma unroll
        for (int r = 0; r < 16; ++r) { const unsigned j = 32 * t + 16 * (r >> 3) + (r & 7) - lo; e[r] = __builtin_amdgcn_exp2f(j < 256u ? z[r] - m : -1e30f); }
        pv(t, (u32x4){pk2(e[0], e[1]), pk2(e[2], e[3]), pk2(e[4], e[5]), pk2(e[6], e[7])}, (u32x4){pk2(e[8], e[9]), pk2(e[10], e[11]), pk2(e[12], e[13]), pk2(e[14], e[15])});
      }
      lsum = __shfl(o[10], i32);
    }

    const float inv = __builtin_amdgcn_rcpf(lsum);
    const int tok = 256 * tb + 32 * wid + i32;
    st16_wt(ws + WS_OMAIN + ((size_t)(tok >> 4) * 16 + (head >> 1)) * 1024 + (2 * (head & 1) + h) * 256 + (tok & 15) * 16,
            (u32x4){pk2(o[0] * inv, o[1] * inv), pk2(o[2] * inv, o[3] * inv), pk2(o[4] * inv, o[5] * inv), pk2(o[6] * inv, o[7] * inv)});
    {
      const int tl = (int)pk2(o[8] * inv, o[9] * inv);
      const u32x4 t4 = {(unsigned)tl, (unsigned)__builtin_amdgcn_update_dpp(0, tl, 0x55, 0xf, 0xf, true), (unsigned)__builtin_amdgcn_update_dpp(0, tl, 0xaa, 0xf, 0xf, true),
                        (unsigned)__builtin_amdgcn_update_dpp(0, tl, 0xff, 0xf, 0xf, true)};
      if ((l & 35) == 0) st16_wt(ws + WS_OTAIL + ((size_t)head * 2048 + tok) * 4, t4);
    }
  }
  const unsigned vb1 = (4 * lw + g) * 9216 + ((i16 ^ ((4 * (lw & 1) + g) << 1)) * 16);
  const char* pb1 = (const char*)w1 + m_ft * 256;
  unsigned lb1 = sb + G1_B + lw * 1024, ob1 = rot1 * 64 * 9216;
  auto issueB1 = [&]() {
    glds<0, W1F>(pb1 + ob1, vb1, lb1);
    ob1 = ob1 == 127 * 16 * 9216 ? 0 : ob1 + 16 * 9216;
    lb1 = lb1 >= sb + G1_B + 27 * 4096 ? lb1 - 27 * 4096 : lb1 + 4096;
  };
  wait_lgkm0();
  if (EB) bar();
  if (EB && wid >= 4 && wg < 192) {
#pragma unroll 1
    for (int j = 0; j < EB; ++j) issueB1();
    wait_vm<EB>();
  } else wait_vm<0>();
  bar();
  if (tid == 0) st8_wt(ws + WS_F_ATT + ((idx >> 2) * 32 + xcd * 4 + (idx & 3)) * FPI, tag);

  if (PFB && wg >= 192) {
    const unsigned junk = sb + 159744 + wid * 256;
    constexpr int D1 = PFA + 32 + 8 * ATN, N0 = W1E - D1, N1 = W2E + 320 - D1;
    auto pf = [&](int i) {
      const char* a = i < N0 || i >= N0 + W2E ? ad_w1(D1 + (i < N0 ? i : i - W2E)) : ad_w2(i - N0 + 288 - W2E);
      touch(a, junk); wait_vm<0>();
    };
    int i = (idx - 24) * 8 + wid;
#pragma unroll 1
    for (int r = 0; r < PFH; ++r, i += 64) pf(i);
    if (wg >= 216) {
#pragma unroll 1
      for (i -= 24; i < N1; i += 40) pf(i);
      if (wid < 4) {
        if (PFI > 1 && idx == 27) pf_code(wid, junk);
        const char* f = ws + WS_F_ML1 + ((9 * kq + (rot2 >> 1)) * 6 + (3 * dt) / 2) * FPI;
#pragma unroll 1
        for (i = (idx - 27) * 4 + wid; i < 288 - W2E; i += 20) {
          touch(ad_w2(i), junk);
          const u32x2 v = ld8_dev(f);
          if (__builtin_amdgcn_readfirstlane(v[0]) == tag[0]) break;
        }
      }
    }
    wait_vm<0>();
  }
  if (wid >= 4) {
    if (wg < 192) {
#pragma unroll 1
      for (int j = EB; j < 12; ++j) issueB1();
      if (lw == 0 && l < 32) pollp<PP & 2, DL2>(ws + WS_F_ATT + ((p_tt >> 2) * 32 + l) * FPI, tag);
      bar();
      const char* bo = ws + WS_OMAIN + (size_t)(4 * p_tt + lw) * 16384;
      const unsigned lo = sb + P_O + lw * 16384;
#pragma unroll
      for (int k = 0; k < 4; ++k) {
        glds<0, 2>(bo + k * 4096, l * 16, lo + k * 4096);
        glds<1024, 2>(bo + k * 4096, l * 16, lo + k * 4096);
        glds<2048, 2>(bo + k * 4096, l * 16, lo + k * 4096);
        glds<3072, 2>(bo + k * 4096, l * 16, lo + k * 4096);
      }
      const char* bt = ws + WS_OTAIL + p_tt * 256;
      const unsigned v = (8 * lw + g) * 8192 + i16 * 16;
      glds<0, 2>(bt, v, sb + P_O + 65536 + lw * 2048);
      glds<0, 2>(bt + 4 * 8192, v, sb + P_O + 65536 + lw * 2048 + 1024);
      wait_vm<14>(); bar();
      wait_vm<10>(); bar();
      wait_vm<5>(); bar();
      wait_vm<0>(); bar();
      bar();
#pragma unroll 1
      for (int j = 0; j < BS1; ++j) issueB1();
      if (EB2) {
        bar();
#pragma unroll 1
        for (int j = 0; j < EB2; ++j) issueB1();
      }
      bar();
      if (tid == 256) st8_wt(ws + WS_F_PRJ + (p_tt * 6 + p_ct) * FPI, tag);
#pragma unroll 1
      for (int j = BS1 + EB2; j < 16; ++j) issueB1();
    } else if (wg < 216) {
#pragma unroll 1
      for (int j = 0; j < 28; ++j) issueB1();
    }
    if (wg < 216) {
      const char* fp = ws + WS_F_PRJ + (((l + rot1) & 31) * 6 + m_dt) * FPI;
      if (lw == 0 && l < 4) pollp<PP & 4, DL4>(fp, tag);
      bar();
      const char* pa = ws + WS_HNT + (size_t)(mh * 36 + 6 * m_dt + 3 * nh) * 1024;
      unsigned sa = rot1;
      auto issueA = [&]() {
        const unsigned la = sb + G1_A + (sa & 3) * 12288 + lw * 3072;
        const char* a = pa + sa * (72 * 1024);
        glds<0, 2>(a, l * 16, la);
        glds<1024, 2>(a, l * 16, la);
        glds<2048, 2>(a, l * 16, la);
        sa = (sa + 1) & 31;
      };
      issueA(); issueA(); issueA(); issueA();
      wait_vm<9>(); bar();
      if (lw == 0) {
        u32x2 fv;
        asm volatile("global_load_dwordx2 %0, %1, off sc1" : "=&v"(fv) : "v"(fp) : "memory");
        wait_vm<7>(); bar();
        issueB1(); issueB1(); issueB1();
        asm volatile("s_waitcnt vmcnt(3)" : "+v"(fv)::"memory");
        if (l < 32 && !(fv[0] == tag[0] && fv[1] == tag[1])) poll(fp, tag);
        if (wg == 0) { if (l == 0) st8_wt(ws, tag); wait_vm<0>(); }
      } else {
        wait_vm<6>(); bar();
        issueB1(); issueB1(); issueB1();
        wait_vm<6>();
      }
      bar();
      issueA(); issueB1(); issueB1(); issueB1(); issueB1(); issueA();
#pragma unroll 1
      for (int s = 0; s < 11; ++s) {

        wait_vm<13>(); bar();
        issueB1(); issueB1(); issueB1(); issueA();
        wait_vm<13>(); bar();
        issueB1(); issueB1(); issueB1(); issueB1(); issueA();
      }
#pragma unroll 1
      for (int s = 0; s < 4; ++s) {
        wait_vm<13>(); bar();
        issueB1(); issueB1(); issueB1(); issueB1(); issueA();
      }
      wait_vm<14>(); bar();
      if (PFC) {
        pf_w2(min(idx * 3 + min(lw, 2), 79), sb + G1_A + lw * 256);
        wait_vm<8>(); bar();
        wait_vm<1>(); bar();
      } else {
        wait_vm<7>(); bar();
        wait_vm<0>(); bar();
      }
      bar();
    }
  } else {
    if (wg < 192) {
      const int tt = p_tt, ct = p_ct;
      f32x8 acc[3];
      {
        u32x4 s0, s1;
#pragma unroll
        for (int e = 0; e < 4; ++e) {
          const int k = 8 * g + 2 * e;
          s0[e] = (k == i16 ? 0x3f80u : 0u) | (k + 1 == i16 ? 0x3f800000u : 0u);
          s1[e] = (k == i16 + 16 ? 0x3f80u : 0u) | (k + 1 == i16 + 16 ? 0x3f800000u : 0u);
        }
        const bf16x8 lo = __builtin_bit_cast(bf16x8, s0), hi = __builtin_bit_cast(bf16x8, s1);
        const f32x4 z = {0, 0, 0, 0};
        f32x4 t[6];
#pragma unroll
        for (int m = 0; m < 2; ++m) { t[3 * m] = mfma16(xr[m][0], lo, z); t[3 * m + 1] = mfma16(xr[m][0], hi, z); t[3 * m + 2] = mfma16(xr[m][1], hi, z); }
#pragma unroll
        for (int k = 0; k < 3; ++k) acc[k] = __builtin_shufflevector(t[2 * k], t[2 * k + 1], 0, 1, 2, 3, 4, 5, 6, 7);
      }
      {
        unsigned aw[3];
#pragma unroll
        for (int c = 0; c < 3; ++c) aw[c] = sb + P_WO + (4 * g + q) * 192 + (((3 * nh + c) ^ (g & 1)) * 32) + 8 * p;
        asm volatile(PJ_ASM : "+{v[40:47]}"(acc[0]), "+{v[48:55]}"(acc[1]), "+{v[56:63]}"(acc[2])
                     : [t0] "v"(aw[0]), [t1] "v"(aw[1]), [t2] "v"(aw[2]), [u0] "v"(aw[0] + 61440), [u1] "v"(aw[1] + 61440), [u2] "v"(aw[2] + 61440),
                       [ap] "v"(sb + P_O + 2 * mh * 16384 + l * 16), [at] "v"(sb + P_O + 65536 + 4 * g * 256 + (32 * mh + i16) * 4)
                     : "memory", PJ_CLOB);
      }
#define HACC(m, c, j) acc[(3 * (m) + (c)) >> 1][4 * ((3 * (m) + (c)) & 1) + (j)]
      char* stats = ws + WS_STATS + (size_t)(2 * tt + mh) * 12 * SPI;
      {
        float rs[8], rq[8];
#pragma unroll
        for (int e = 0; e < 8; ++e) {
          const float a = HACC(e >> 2, 0, e & 3), b = HACC(e >> 2, 1, e & 3), c = HACC(e >> 2, 2, e & 3);
          rs[e] = a + b + c; rq[e] = a * a + b * b + c * c;
        }
        asm volatile(
                     "s_nop 1\n\t"
                     "v_add_f32_dpp %0, %0, %0 row_ror:8 row_mask:0xf bank_mask:0xf\n\t"
                     "v_add_f32_dpp %1, %1, %1 row_ror:8 row_mask:0xf bank_mask:0xf\n\t"
                     "v_add_f32_dpp %2, %2, %2 row_ror:8 row_mask:0xf bank_mask:0xf\n\t"
                     "v_add_f32_dpp %3, %3, %3 row_ror:8 row_mask:0xf bank_mask:0xf\n\t"
                     "v_add_f32_dpp %8, %8, %8 row_ror:8 row_mask:0xf bank_mask:0xf\n\t"
                     "v_add_f32_dpp %9, %9, %9 row_ror:8 row_mask:0xf bank_mask:0xf\n\t"
                     "v_add_f32_dpp %10, %10, %10 row_ror:8 row_mask:0xf bank_mask:0xf\n\t"
                     "v_add_f32_dpp %11, %11, %11 row_ror:8 row_mask:0xf bank_mask:0xf\n\t"
                     "v_add_f32_dpp %0, %4, %4 row_ror:8 row_mask:0xf bank_mask:0xc\n\t"
                     "v_add_f32_dpp %1, %5, %5 row_ror:8 row_mask:0xf bank_mask:0xc\n\t"
                     "v_add_f32_dpp %2, %6, %6 row_ror:8 row_mask:0xf bank_mask:0xc\n\t"
                     "v_add_f32_dpp %3, %7, %7 row_ror:8 row_mask:0xf bank_mask:0xc\n\t"
                     "v_add_f32_dpp %8, %12, %12 row_ror:8 row_mask:0xf bank_mask:0xc\n\t"
                     "v_add_f32_dpp %9, %13, %13 row_ror:8 row_mask:0xf bank_mask:0xc\n\t"
                     "v_add_f32_dpp %10, %14, %14 row_ror:8 row_mask:0xf bank_mask:0xc\n\t"
                     "v_add_f32_dpp %11, %15, %15 row_ror:8 row_mask:0xf bank_mask:0xc\n\t"
                     "v_add_f32_dpp %0, %0, %0 row_half_mirror row_mask:0xf bank_mask:0xf\n\t"
                     "v_add_f32_dpp %1, %1, %1 row_half_mirror row_mask:0xf bank_mask:0xf\n\t"
                     "v_add_f32_dpp %8, %8, %8 row_half_mirror row_mask:0xf bank_mask:0xf\n\t"
                     "v_add_f32_dpp %9, %9, %9 row_half_mirror row_mask:0xf bank_mask:0xf\n\t"
                     "v_add_f32_dpp %0, %2, %2 row_half_mirror row_mask:0xf bank_mask:0xa\n\t"
                     "v_add_f32_dpp %1, %3, %3 row_half_mirror row_mask:0xf bank_mask:0xa\n\t"
                     "v_add_f32_dpp %8, %10, %10 row_half_mirror row_mask:0xf bank_mask:0xa\n\t"
                     "v_add_f32_dpp %9, %11, %11 row_half_mirror row_mask:0xf bank_mask:0xa\n\t"
                     "v_add_f32_dpp %0, %0, %0 quad_perm:[1,0,3,2] row_mask:0xf bank_mask:0xf\n\t"
                     "v_add_f32_dpp %1, %1, %1 quad_perm:[1,0,3,2] row_mask:0xf bank_mask:0xf\n\t"
                     "v_add_f32_dpp %8, %8, %8 quad_perm:[1,0,3,2] row_mask:0xf bank_mask:0xf\n\t"
                     "v_add_f32_dpp %9, %9, %9 quad_perm:[1,0,3,2] row_mask:0xf bank_mask:0xf\n\t"
                     "v_add_f32_dpp %0, %0, %0 quad_perm:[2,3,0,1] row_mask:0xf bank_mask:0xf\n\t"
                     "v_add_f32_dpp %1, %1, %1 quad_perm:[2,3,0,1] row_mask:0xf bank_mask:0xf\n\t"
                     "v_add_f32_dpp %8, %8, %8 quad_perm:[2,3,0,1] row_mask:0xf bank_mask:0xf\n\t"
                     "v_add_f32_dpp %9, %9, %9 quad_perm:[2,3,0,1] row_mask:0xf bank_mask:0xf\n\t"
                     "s_nop 0" : "+v"(rs[0]), "+v"(rs[1]), "+v"(rs[2]), "+v"(rs[3]), "+v"(rs[4]), "+v"(rs[5]), "+v"(rs[6]), "+v"(rs[7]), "+v"(rq[0]), "+v"(rq[1]), "+v"(rq[2]), "+v"(rq[3]), "+v"(rq[4]), "+v"(rq[5]), "+v"(rq[6]), "+v"(rq[7]));
        if (p < 2) st16_wt(stats + (2 * ct + nh) * SPI + (16 * (q >> 1) + 4 * g + 2 * (q & 1) + p) * 16,
                           (u32x4){__float_as_uint(p ? rs[1] : rs[0]), __float_as_uint(p ? rq[1] : rq[0]), tag[0], tag[1]});
      }
      const size_t fo = ((size_t)(2 * tt + mh) * 36 + 6 * ct + 3 * nh) * 1024 + l * 16;
      auto store_h = [&]() {
#pragma unroll
      for (int c = 0; c < 3; ++c)
        st16_wt(ws + WS_HT + fo + c * 1024, (u32x4){pk2(HACC(0, c, 0), HACC(0, c, 1)), pk2(HACC(0, c, 2), HACC(0, c, 3)), pk2(HACC(1, c, 0), HACC(1, c, 1)), pk2(HACC(1, c, 2), HACC(1, c, 3))});
      };
      if (HL == 0) store_h();
      u32x4 gv[6];
      {
        const char* src = stats + (6 * (l >> 5) + 2) * SPI + (l & 31) * 16;
        if (SS0) __builtin_amdgcn_s_sleep(SS0);
      #pragma unroll 1
  for (int spin = 0; spin < SPIN_MAX; ++spin) {
          asm volatile("global_load_dwordx4 %0, %6, off offset:%7 sc1\n\tglobal_load_dwordx4 %1, %6, off offset:%8 sc1\n\tglobal_load_dwordx4 %2, %6, off sc1\n\t"
                       "global_load_dwordx4 %3, %6, off offset:%9 sc1\n\tglobal_load_dwordx4 %4, %6, off offset:%10 sc1\n\tglobal_load_dwordx4 %5, %6, off offset:%11 sc1\n\t"
                       "s_waitcnt vmcnt(0)"
                       : "=&v"(gv[0]), "=&v"(gv[1]), "=&v"(gv[2]), "=&v"(gv[3]), "=&v"(gv[4]), "=&v"(gv[5]) : "v"(src), "i"(-2 * SPI), "i"(-SPI), "i"(SPI), "i"(2 * SPI), "i"(3 * SPI) : "memory");
          bool ok = true;
#pragma unroll
          for (int k = 0; k < 6; ++k) ok &= gv[k][2] == tag[0] && gv[k][3] == tag[1];
          if (__all(ok)) break;
          if (SS1) __builtin_amdgcn_s_sleep(SS1);
        }
      }
      {
        float s = 0, m2 = 0;
#pragma unroll
        for (int k = 0; k < 6; ++k) { s += __uint_as_float(gv[k][0]); m2 += __uint_as_float(gv[k][1]); }
        {
          const auto a = __builtin_amdgcn_permlane32_swap(__float_as_uint(s), __float_as_uint(s), false, false);
          const auto b = __builtin_amdgcn_permlane32_swap(__float_as_uint(m2), __float_as_uint(m2), false, false);
          s = __uint_as_float(a[0]) + __uint_as_float(a[1]); m2 = __uint_as_float(b[0]) + __uint_as_float(b[1]);
        }
        const float mu = s * (1.f / 576.f);
        const float var = fmaxf(m2 * (1.f / 576.f) - mu * mu, 0.f);
        const float r1 = __builtin_amdgcn_rsqf(var + 1e-5f);
        const float r2 = __builtin_amdgcn_rsqf(var * r1 * r1 + 1e-5f);
        const unsigned a = sb + P_AUX + wid * 256;
        if (l < 32) *(LDS3 f32x2*)(a + l * 8) = (f32x2){-mu * r1 * r2, r1 * r2};
        wait_lgkm0();
        f32x4 st[2][2];
#pragma unroll
        for (int m = 0; m < 2; ++m)
#pragma unroll
          for (int k = 0; k < 2; ++k) st[m][k] = *(const LDS3 f32x4*)(a + (16 * m + 4 * g + 2 * k) * 8);
#pragma unroll
        for (int c = 0; c < 3; ++c) {
          float v[8];
#pragma unroll
          for (int e = 0; e < 8; ++e) v[e] = __builtin_fmaf(HACC(e >> 2, c, e & 3), st[e >> 2][(e >> 1) & 1][(e & 1) * 2 + 1], st[e >> 2][(e >> 1) & 1][(e & 1) * 2]);
          st16_wt(ws + WS_HNT + fo + c * 1024, (u32x4){pk2(v[0], v[1]), pk2(v[2], v[3]), pk2(v[4], v[5]), pk2(v[6], v[7])});
        }
      }
      if (HL == 1) store_h();
      if (EB2) bar();
      wait_vm<HL == 1 ? 3 : 0>(); wait_lgkm0(); bar();
      if (HL == 2) store_h();
    }
    if (wg < 216) {
      f32x16 acc[3];
      {
        float bv[16];
#pragma unroll
        for (int c = 0; c < 4; ++c) {
          const u32x2 b = *(const u32x2*)(b1 + 128 * m_ft + 64 * nh + 16 * c + 4 * g);
          bv[4 * c] = bflo(b[0]); bv[4 * c + 1] = bfhi(b[0]); bv[4 * c + 2] = bflo(b[1]); bv[4 * c + 3] = bfhi(b[1]);
        }
#pragma unroll
        for (int i = 0; i < 48; ++i) acc[i >> 4][i & 15] = bv[4 * (i / 12) + (i & 3)];
      }
      unsigned ap = sb + G1_A + 3 * mh * 1024 + l * 16, aw[4];
#pragma unroll
      for (int c = 0; c < 4; ++c) aw[c] = sb + G1_B + (4 * g + q) * 256 + (((4 * nh + c) ^ (4 * (g & 1) + q)) * 32) + 8 * p;
      bar();
      unsigned cnt = 31, ta, tb;
      asm volatile(G1_ASM : "+{v[64:79]}"(acc[0]), "+{v[80:95]}"(acc[1]), "+{v[96:111]}"(acc[2]), [ap] "+v"(ap),
                   [t0] "+v"(aw[0]), [t1] "+v"(aw[1]), [t2] "+v"(aw[2]), [t3] "+v"(aw[3]), [cnt] "+s"(cnt), [ta] "=&s"(ta), [tb] "=&s"(tb)
                   : [ma] "s"(wrap_mask(32, 4)), [sa] "s"(12288), [wa] "s"(-3 * 12288), [mb] "s"(wrap_mask(32, 7)), [sb] "s"(16384), [wb] "s"(-6 * 16384)
                   : "memory", "scc", G1_CLOB);
      if (B2E) { bb0 = *(const u32x2*)(b2 + 128 * st + 32 * lw + 4 * g); bb1 = *(const u32x2*)(b2 + 128 * st + 32 * lw + 16 + 4 * g); }
#pragma unroll
      for (int a = 0; a < 2; ++a)
#pragma unroll
        for (int u = 0; u < 3; ++u) {
          u32x4 o;
#pragma unroll
          for (int e = 0; e < 4; ++e) { const int i = ((2 * a + (e >> 1)) * 3 + u) * 4 + 2 * (e & 1); o[e] = gelu2((f32x2){acc[i >> 4][i & 15], acc[i >> 4][(i & 15) + 1]}); }
          st16_s<0>(ws + WS_UT + ((size_t)(4 * m_ft + 2 * nh + a) * 36 + 6 * m_dt + 3 * mh + u) * 1024, l * 16, o);
        }
    }
  }

  if (wid >= 4) {
    const unsigned vb = (4 * lw + g) * 4096 + ((i16 ^ ((4 * (lw & 1) + g) << 1)) * 16);
    const char* pb = (const char*)w2 + (size_t)kq * 1152 * 4096 + st * 256;
    unsigned lb = sb + G2_B + lw * 1024, ob = rot2 * 64 * 4096;
    auto issueB = [&]() {
#pragma unroll
      for (int i = 0; i < 4; ++i) glds<0, W2F>(pb + ob + i * 16 * 4096, vb, lb + i * 4096);
      ob = ob == 17 * 64 * 4096 ? 0 : ob + 64 * 4096;
      lb = lb >= sb + G2_B + 4 * 16384 ? lb - 4 * 16384 : lb + 16384;
    };
#pragma unroll 1
    for (int s = 0; s < PB1; ++s) issueB();
    if (PB1 < 5) {
      bar();
#pragma unroll 1
      for (int s = PB1; s < 5; ++s) issueB();
    }
    bar();
    const int fj = (min(l, 17) + rot2) >> 1;
    const char* fp = ws + WS_F_ML1 + ((9 * kq + (fj >= 9 ? fj - 9 : fj)) * 6 + (3 * dt) / 2 + (l & 1)) * FPI;
    if (lw == 0 && l < 4) {
      if (wg >= 216 && l == 0) poll(fp, tag);
      pollp<PP & 8, DL8>(fp, tag);
    }
    bar();
    const int u0 = mh * 5;
    const char* pa = ws + WS_UT + (size_t)((36 * kq + nh) * 36 + 9 * dt + u0) * 1024;
    unsigned sa = 0, oa = rot2 * 72 * 1024;
    auto issueA = [&]() {
      const unsigned la = sb + G2_A + sa * 18432 + (9 * nh + u0) * 1024;
      const char* a = pa + oa;
      glds<0, 2>(a, l * 16, la);
      glds<1024, 2>(a, l * 16, la);
      glds<2048, 2>(a, l * 16, la);
      glds<3072, 2>(a, l * 16, la);
      if (mh == 0) glds<0, 2>(a + 4096, l * 16, la + 4096);
      oa = oa == 17 * 72 * 1024 ? 0 : oa + 72 * 1024; sa = (sa + 1) & 3;
    };
    auto waitx = [&](auto na, auto nb, auto nx) { if (mh == 0) wait_vm<5 * na.value + 4 * nb.value + nx.value>(); else wait_vm<4 * na.value + 4 * nb.value + nx.value>(); bar(); };
    auto waitn = [&](auto na, auto nb) { waitx(na, nb, std::integral_constant<int, 0>{}); };
    using I0 = std::integral_constant<int, 0>; using I1 = std::integral_constant<int, 1>; using I2 = std::integral_constant<int, 2>; using I3 = std::integral_constant<int, 3>;
    issueA(); issueA(); issueA(); issueA();
    waitn(I3{}, I0{});
    if (lw == 0) {
      u32x2 fv;
      asm volatile("global_load_dwordx2 %0, %1, off sc1" : "=&v"(fv) : "v"(fp) : "memory");
      wait_vm<11>(); bar();
      issueB();
      asm volatile("s_waitcnt vmcnt(4)" : "+v"(fv)::"memory");
      if (l < 18 && !(fv[0] == tag[0] && fv[1] == tag[1])) poll(fp, tag);
      bar();
    } else {
      waitn(I2{}, I0{});
      issueB();
      waitn(I1{}, I1{});
    }
    issueA();
#pragma unroll 1
    for (int s = 0; s < 12; ++s) { issueB(); issueA(); waitn(I2{}, I2{}); }
    issueA();
    {
      const char* hT = ws + WS_HT + ((size_t)(4 * st + kq) * 36 + 9 * dt + 2 * lw) * 1024;
      const unsigned lh = sb + F_H + 2 * lw * 1024;
      glds<0, 2>(hT, l * 16, lh); glds<1024, 2>(hT, l * 16, lh); glds<2048, 2>(hT, l * 16, lh);
    }
    if (PFI > 1 && lw == 0 && idx < 4) { waitx(I2{}, I1{}, I3{}); pf_code(0, sb + 159744); wait_vm<9>(); bar(); wait_vm<4>(); bar(); wait_vm<1>(); bar(); }
    else {
    waitx(I2{}, I1{}, I3{});
    waitx(I1{}, I0{}, I3{});
    waitx(I0{}, I0{}, I3{});
    waitn(I0{}, I0{});
    }
  } else {
    if (!B2E || wg >= 216) { bb0 = *(const u32x2*)(b2 + 128 * st + 32 * lw + 4 * g); bb1 = *(const u32x2*)(b2 + 128 * st + 32 * lw + 16 + 4 * g); }
    if (PB1 < 5) bar();
    wait_vm<0>(); bar();
    if (tid == 0 && wg < 216) st8_wt(ws + WS_F_ML1 + m_tile * FPI, tag);
    f32x16 acc[4]; f32x8 acc4;
    if (wid != kq) bb0 = bb1 = (u32x2){0, 0};
#pragma unroll
    for (int i = 0; i < 72; ++i) {
      const unsigned w = (i < 36 ? bb0 : bb1)[(i >> 1) & 1];
      const float v = (i & 1) ? bfhi(w) : bflo(w);
      if (i < 64) acc[i >> 4][i & 15] = v; else acc4[i - 64] = v;
    }
    unsigned ap = sb + G2_A + l * 16, aw[2];
#pragma unroll
    for (int c = 0; c < 2; ++c) aw[c] = sb + G2_B + (4 * g + q) * 256 + (((2 * lw + c) ^ (4 * (g & 1) + q)) * 32) + 8 * p;
    bar();
    unsigned ta, tb;
#define YACC(c, u, j) (((c) * 9 + (u)) < 16 ? acc[(((c) * 9 + (u)) >> 2) & 3][4 * (((c) * 9 + (u)) & 3) + (j)] : acc4[4 * (((c) * 9 + (u)) & 1) + (j)])
    if (wid == kq) {
      unsigned cnt = 17;
      asm volatile(G2_ASM : "+{v[64:79]}"(acc[0]), "+{v[80:95]}"(acc[1]), "+{v[96:111]}"(acc[2]), "+{v[112:127]}"(acc[3]), "+{v[128:135]}"(acc4), [ap] "+v"(ap),
                   [t0] "+v"(aw[0]), [t1] "+v"(aw[1]), [cnt] "+s"(cnt), [ta] "=&s"(ta), [tb] "=&s"(tb)
                   : [ma] "s"(wrap_mask(18, 4)), [sa] "s"(18432), [wa] "s"(-3 * 18432), [mb] "s"(wrap_mask(18, 5)), [sb] "s"(16384), [wb] "s"(-4 * 16384)
                   : "memory", "scc", G2_CLOB);
#pragma unroll
      for (int u = 0; u < 9; ++u) {
        *(LDS3 f32x4*)(sb + u * 2048 + l * 16) = (f32x4){YACC(0, u, 0), YACC(0, u, 1), YACC(0, u, 2), YACC(0, u, 3)};
        *(LDS3 f32x4*)(sb + u * 2048 + 1024 + l * 16) = (f32x4){YACC(1, u, 0), YACC(1, u, 1), YACC(1, u, 2), YACC(1, u, 3)};
      }
    } else {
      unsigned cnt = 16;
      const char* sp = part + (wid * 3 + ((kq - wid - 1) & 3)) * 9216 + 4096;
      asm volatile(G2F_ASM : "+{v[64:79]}"(acc[0]), "+{v[80:95]}"(acc[1]), "+{v[96:111]}"(acc[2]), "+{v[112:127]}"(acc[3]), "+{v[128:135]}"(acc4), [ap] "+v"(ap),
                   [t0] "+v"(aw[0]), [t1] "+v"(aw[1]), [cnt] "+s"(cnt), [ta] "=&s"(ta), [tb] "=&s"(tb)
                   : [ma] "s"(wrap_mask(17, 4)), [sa] "s"(18432), [wa] "s"(-3 * 18432), [mb] "s"(wrap_mask(17, 5)), [sb] "s"(16384), [wb] "s"(-4 * 16384),
                     [vo] "v"(l * 16), [sp] "s"(sp), [sq] "s"(sp + 4096)
                   : "memory", "scc", G2F_CLOB);
      wait_vm<0>();
      if (l == 0) st8_wt(ws + WS_F_RED + (tile * 16 + wid * 4 + kq) * FPI, tag);
    }
  }
  if (wid == 6 && l < 4 && l != kq) pollp<PP & 16>(ws + WS_F_RED + (tile * 16 + kq * 4 + l) * FPI, tag);
  wait_lgkm0(); bar();
  const unsigned tb_ = sb + 32768;
  auto fin0 = [&](int u, float* v) {
    const f32x4 a0 = *(const LDS3 f32x4*)(sb + u * 2048 + l * 16);
    const f32x4 a1 = *(const LDS3 f32x4*)(sb + u * 2048 + 1024 + l * 16);
    const u32x4 hv = *(const LDS3 u32x4*)(sb + F_H + u * 1024 + l * 16);
#pragma unroll
    for (int e = 0; e < 8; ++e) v[e] = (e < 4 ? a0[e] : a1[e - 4]) + ((e & 1) ? bfhi(hv[e >> 1]) : bflo(hv[e >> 1]));
  };
  auto fin1 = [&](int u, float* v, const u32x4* pv) {
#pragma unroll
    for (int e = 0; e < 8; ++e) {
      const int w = e >> 1;
      if (e & 1) v[e] = (v[e] + bfhi(pv[0][w])) + (bfhi(pv[1][w]) + bfhi(pv[2][w]));
      else v[e] = (v[e] + bflo(pv[0][w])) + (bflo(pv[1][w]) + bflo(pv[2][w]));
    }
    const unsigned wa = tb_ + (16 * u + i16) * 72 + 8 * g;
    *(LDS3 u32x2*)(wa) = (u32x2){pk2(v[0], v[1]), pk2(v[2], v[3])};
    *(LDS3 u32x2*)(wa + 32) = (u32x2){pk2(v[4], v[5]), pk2(v[6], v[7])};
  };
  auto fin2 = [&](int u) {
    const unsigned r = tb_ + (16 * u + 8 * (g >> 1) + q) * 72 + 32 * (g & 1) + 8 * p;
    return cat(lds_tr(r), lds_tr(r + 4 * 72));
  };
  {
    const char* in0 = part + kq * 3 * 9216;
    const unsigned vo = wid * 1024 + l * 16;
    u16* po = out + (size_t)(128 * st + 32 * kq + 16 * (g & 1) + i16) * 576 + 144 * dt + 16 * wid + 8 * (g >> 1);
    u32x4 pv[6];
    float v[16];
#define V8(o) "+v"(v[o]), "+v"(v[o + 1]), "+v"(v[o + 2]), "+v"(v[o + 3]), "+v"(v[o + 4]), "+v"(v[o + 5]), "+v"(v[o + 6]), "+v"(v[o + 7])
    if (wid != 7) {
      asm volatile("global_load_dwordx4 %0, %3, %4 sc1\n\tglobal_load_dwordx4 %1, %3, %5 sc1\n\tglobal_load_dwordx4 %2, %3, %6 sc1"
                   : "=&v"(pv[0]), "=&v"(pv[1]), "=&v"(pv[2]) : "v"(vo), "s"(in0), "s"(in0 + 9216), "s"(in0 + 18432) : "memory");
      fin0(wid, v);
      asm volatile("s_waitcnt vmcnt(0)" : "+v"(pv[0]), "+v"(pv[1]), "+v"(pv[2]), V8(0), "+v"(po)::"memory");
      fin1(wid, v, pv);
      wait_lgkm0();
      st_out(po, fin2(wid));
    } else {
      asm volatile("global_load_dwordx4 %0, %6, %7 sc1\n\tglobal_load_dwordx4 %1, %6, %8 sc1\n\tglobal_load_dwordx4 %2, %6, %9 sc1\n\t"
                   "global_load_dwordx4 %3, %6, %7 offset:1024 sc1\n\tglobal_load_dwordx4 %4, %6, %8 offset:1024 sc1\n\tglobal_load_dwordx4 %5, %6, %9 offset:1024 sc1"
                   : "=&v"(pv[0]), "=&v"(pv[1]), "=&v"(pv[2]), "=&v"(pv[3]), "=&v"(pv[4]), "=&v"(pv[5]) : "v"(vo), "s"(in0), "s"(in0 + 9216), "s"(in0 + 18432) : "memory");
      fin0(7, v); fin0(8, v + 8);
      asm volatile("s_waitcnt vmcnt(0)" : "+v"(pv[0]), "+v"(pv[1]), "+v"(pv[2]), "+v"(pv[3]), "+v"(pv[4]), "+v"(pv[5]), V8(0), V8(8), "+v"(po)::"memory");
      fin1(7, v, pv); fin1(8, v + 8, pv + 3);
      wait_lgkm0();
      const bf16x8 o0 = fin2(7), o1 = fin2(8);
      st_out(po, o0); st_out(po + 16, o1);
    }
  }

  asm volatile("mega_code_end:");
}

extern "C" void kernel_launch(void* const* d_in, const int* in_sizes, int n_in, void* d_out, int out_size, void* d_ws, size_t ws_size,
                              hipStream_t stream) {
  (void)hipFuncSetAttribute((const void*)mega, hipFuncAttributeMaxDynamicSharedMemorySize, LDS_BYTES);
  mega<<<256, 512, LDS_BYTES, stream>>>((const u16*)d_in[0], (const u16*)d_in[1], (const u16*)d_in[2], (const u16*)d_in[4], (const u16*)d_in[5],
                                        (const u16*)d_in[6], (const u16*)d_in[7], (u16*)d_out, (char*)d_ws);
}
```
